```python
import torch

def _softmax(x, axis=-1):
    return torch.softmax(x, dim=axis)
def _gelu(x):
    return torch.nn.functional.gelu(x, approximate="tanh")
def _silu(x):
    return torch.nn.functional.silu(x)
def _sigmoid(x):
    return torch.sigmoid(x)
def _layernorm(x, axis=-1, eps=1e-5):
    m = x.mean(dim=axis, keepdim=True)
    v = x.var(dim=axis, keepdim=True, unbiased=False)
    return (x - m) / torch.sqrt(v + eps)
def _scatter_add(tgt, idx, src, axis=0):
    return tgt.clone().index_add_(axis, idx, src)
def _segment_sum(src, seg, n):
    out = torch.zeros((n,) + src.shape[1:], dtype=src.dtype)
    return out.index_add_(0, seg, src)
def _pad(x, pw):
    flat = tuple(v for pair in reversed(pw) for v in pair)
    return torch.nn.functional.pad(x, flat)

kernel_name = 'recurrent_scan__ssm_conv_s5__mixer_s_mlp__prompt'

def setup_inputs():

    _w = (lambda a: (lambda t: t.to(torch.bfloat16) if t.is_floating_point() else t)(torch.as_tensor(a)))
    def _A_setup():
        B, S, D = 1, 2048, 512
        P = D
        Lambda_re = -0.5 * (lambda _x: torch.exp(torch.as_tensor(_x)))((lambda *s: torch.randn(*s, dtype=torch.bfloat16) * 0.2)(P))
        Lambda_im = (lambda *s: torch.randn(*s, dtype=torch.bfloat16) * 0.4)(P) * 1.0
        B_re = (lambda *s: torch.randn(*s, dtype=torch.bfloat16) * 0.2)(P, D) * (D ** -0.5)
        B_im = (lambda *s: torch.randn(*s, dtype=torch.bfloat16) * 0.2)(P, D) * (D ** -0.5)
        C_re = (lambda *s: torch.randn(*s, dtype=torch.bfloat16) * 0.2)(D, P) * (P ** -0.5)
        C_im = (lambda *s: torch.randn(*s, dtype=torch.bfloat16) * 0.2)(D, P) * (P ** -0.5)
        log_dt = (lambda *s: torch.randn(*s, dtype=torch.bfloat16) * 0.2)(P) * 0.3 - 2.0
        Dskip = (lambda *s: torch.randn(*s, dtype=torch.bfloat16) * 0.05)(D)
        return {
            "x": _w((lambda *s: torch.randn(*s, dtype=torch.bfloat16) * 0.05)(B, S, D)),
            "Lambda_re": _w(Lambda_re),
            "Lambda_im": _w(Lambda_im),
            "B_re": _w(B_re),
            "B_im": _w(B_im),
            "C_re": _w(C_re),
            "C_im": _w(C_im),
            "log_dt": _w(log_dt),
            "Dskip": _w(Dskip),
        }
    _A_in = _A_setup()

    def _B_setup():
        B, S, D, F = 1, 2048, 512, 1792
        return {
            "x":  _w((lambda *s: torch.randn(*s, dtype=torch.bfloat16) * 0.05)(B, S, D)),
            "w1": _w((lambda *s: torch.randn(*s, dtype=torch.bfloat16) * 3.2)(D, D) * (D ** -0.5)),
            "w2": _w((lambda *s: torch.randn(*s, dtype=torch.bfloat16) * 3.2)(D, D) * (D ** -0.5)),
            "w3": _w((lambda *s: torch.randn(*s, dtype=torch.bfloat16) * 3.2)(D, F) * (D ** -0.5)),
            "w4": _w((lambda *s: torch.randn(*s, dtype=torch.bfloat16) * 3.2)(F, D) * (F ** -0.5)),
        }
    _B_in = _B_setup()

    return {
        "A_x": _A_in["x"],
        "A_Lambda_re": _A_in["Lambda_re"],
        "A_Lambda_im": _A_in["Lambda_im"],
        "A_B_re": _A_in["B_re"],
        "A_B_im": _A_in["B_im"],
        "A_C_re": _A_in["C_re"],
        "A_C_im": _A_in["C_im"],
        "A_log_dt": _A_in["log_dt"],
        "A_Dskip": _A_in["Dskip"],
        "B_x": _B_in["x"],
        "B_w1": _B_in["w1"],
        "B_w2": _B_in["w2"],
        "B_w3": _B_in["w3"],
        "B_w4": _B_in["w4"],
    }

def reference(A_x, A_Lambda_re, A_Lambda_im, A_B_re, A_B_im, A_C_re, A_C_im, A_log_dt, A_Dskip, B_x, B_w1, B_w2, B_w3, B_w4):
    def _A_ref(x, Lambda_re, Lambda_im, B_re, B_im, C_re, C_im, log_dt, Dskip):
        Bsz, L, H = x.shape
        Lam = Lambda_re + 1j * Lambda_im
        Bmat = B_re + 1j * B_im
        Cmat = C_re + 1j * C_im
        dt = torch.exp(log_dt)
        Lam_bar = torch.exp(dt * Lam)
        B_bar = ((Lam_bar - 1.0) / Lam)[:, None] * Bmat
        Bu = torch.einsum("bld,pd->blp", x + 0j, B_bar)
        h = torch.zeros_like(Bu[:, 0, :])
        hs = []
        for t in range(L):
            h = Lam_bar[None, :] * h + Bu[:, t, :]
            hs.append(h)
        hseq = torch.stack(hs, 1)
        y = 2.0 * torch.einsum("blp,dp->bld", hseq, Cmat).real + x * Dskip[None, None, :]
        return y

    def _B_ref(x, w1, w2, w3, w4):
        B, S, D = x.shape
        h = _gelu(_layernorm(x, axis=-1) @ w1)
        c = D // 4
        g0 = _pad(h[:, :, 0*c:1*c], ((0, 0), (1, 0), (0, 0)))[:, :S, :]
        g1 = _pad(h[:, :, 1*c:2*c], ((0, 0), (0, 1), (0, 0)))[:, 1:, :]
        g2 = _pad(h[:, :, 2*c:3*c], ((0, 0), (2, 0), (0, 0)))[:, :S, :]
        g3 = _pad(h[:, :, 3*c:   ], ((0, 0), (0, 2), (0, 0)))[:, 2:, :]
        hs = torch.concatenate([g0, g1, g2, g3], axis=-1) @ w2
        x1 = x + hs
        h2 = _gelu(_layernorm(x1, axis=-1) @ w3) @ w4
        return x1 + h2

    x = A_x
    xn = _layernorm(x, axis=-1)
    s = _A_ref(xn, A_Lambda_re, A_Lambda_im, A_B_re, A_B_im, A_C_re, A_C_im, A_log_dt, A_Dskip)
    g = _silu(_B_ref(xn, B_w1, B_w2, B_w3, B_w4))
    return x + s * g
```

```cpp
#include <hip/hip_runtime.h>
#include <hip/hip_bf16.h>
#include <stdint.h>
typedef unsigned short u16;
static constexpr int S=2048, D=512, P=512, F=1792;
__device__ __forceinline__ float bf2f(u16 v){ return __uint_as_float(((unsigned)v)<<16); }
__device__ __forceinline__ u16 f2bf(float f){ unsigned u=__float_as_uint(f); u += 0x7fffu + ((u>>16)&1u); return (u16)(u>>16); }
__device__ __forceinline__ float rb(float f){ return bf2f(f2bf(f)); }
__device__ __forceinline__ float gelu_t(float x){ float u=0.7978845608028654f*(x+0.044715f*x*x*x); return 0.5f*x*(1.f+tanhf(u)); }

__global__ void ln_k(const u16* in, u16* out){
  int row=blockIdx.x, l=threadIdx.x; float v[8]; float s=0;
  for(int j=0;j<8;j++){ v[j]=bf2f(in[row*D+l*8+j]); s+=v[j]; }
  for(int o=32;o;o>>=1) s+=__shfl_xor(s,o);
  float mean=s/D; float q=0; for(int j=0;j<8;j++){ float d=v[j]-mean; q+=d*d; }
  for(int o=32;o;o>>=1) q+=__shfl_xor(q,o);
  float m=rb(mean), var=rb(q/D); float den=rb(sqrtf(rb(var+1e-5f)));
  for(int j=0;j<8;j++) out[row*D+l*8+j]=f2bf(rb(v[j]-m)/den);
}
template<int MODE> __global__ void gemm_k(const u16* A, const u16* W, float* C, int N, int K){
  int n=blockIdx.x*64+threadIdx.x, m=blockIdx.y; float acc=0;
  for(int k=0;k<K;k++) acc+=bf2f(A[(size_t)m*K+k])*bf2f(MODE==0? W[(size_t)k*N+n]: W[(size_t)n*K+k]);
  C[(size_t)m*N+n]=acc;
}
__global__ void gelu_k(const float* in,u16* out,int n){ int i=blockIdx.x*256+threadIdx.x; if(i<n) out[i]=f2bf(gelu_t(rb(in[i]))); }
__global__ void shift_k(const u16* h,u16* out){ int i=blockIdx.x*256+threadIdx.x; int t=i/D,c=i%D; int g=c/128; int sh= g==0?-1: g==1?1: g==2?-2:2; int ts=t+sh; out[i]=(ts>=0&&ts<S)?h[ts*D+c]:0; }
__global__ void addres_k(const u16* a,const float* b,u16* out,int n){ int i=blockIdx.x*256+threadIdx.x; if(i<n) out[i]=f2bf(bf2f(a[i])+rb(b[i])); }
__global__ void scan_k(const float* bre,const float* bim,const u16* lre,const u16* lim,const u16* ldt,float* hre,float* him){
  int p=blockIdx.x*64+threadIdx.x; float lr=bf2f(lre[p]), li=bf2f(lim[p]); float dt=rb(expf(bf2f(ldt[p])));
  float e=expf(dt*lr); float ar=e*cosf(dt*li), ai=e*sinf(dt*li);
  float nr=ar-1.f, ni=ai; float den=lr*lr+li*li; float cr=(nr*lr+ni*li)/den, ci=(ni*lr-nr*li)/den;
  float hr=0,hi=0;
  for(int t=0;t<S;t++){ float ur=bre[t*P+p], ui=bim[t*P+p]; float br=cr*ur-ci*ui, bi=cr*ui+ci*ur;
    float nhr=ar*hr-ai*hi+br, nhi=ar*hi+ai*hr+bi; hr=nhr; hi=nhi; hre[t*P+p]=hr; him[t*P+p]=hi; }
}
__global__ void yc_k(const float* hre,const float* him,const u16* cre,const u16* cim,float* y){
  int d=blockIdx.x*64+threadIdx.x, t=blockIdx.y; float acc=0;
  for(int p=0;p<P;p++) acc+=hre[t*P+p]*bf2f(cre[d*P+p])-him[t*P+p]*bf2f(cim[d*P+p]);
  y[t*D+d]=acc;
}
__global__ void final_k(const u16* x,const u16* xn,const u16* dsk,const float* y,const u16* x1,const float* h2,float* out){
  int i=blockIdx.x*256+threadIdx.x; int d=i%D;
  float s=2.f*y[i]+rb(bf2f(xn[i])*bf2f(dsk[d]));
  float b=rb(bf2f(x1[i])+rb(h2[i])); float g=rb(b/(1.f+expf(-b)));
  out[i]=bf2f(x[i])+s*g;
}
extern "C" void kernel_launch(void* const* d_in, const int* in_sizes, int n_in, void* d_out, int out_size, void* d_ws, size_t ws_size, hipStream_t stream){
  const u16* x=(const u16*)d_in[0]; const u16* lre=(const u16*)d_in[1]; const u16* lim=(const u16*)d_in[2];
  const u16* bre=(const u16*)d_in[3]; const u16* bim=(const u16*)d_in[4]; const u16* cre=(const u16*)d_in[5]; const u16* cim=(const u16*)d_in[6];
  const u16* ldt=(const u16*)d_in[7]; const u16* dsk=(const u16*)d_in[8];
  const u16* w1=(const u16*)d_in[10]; const u16* w2=(const u16*)d_in[11]; const u16* w3=(const u16*)d_in[12]; const u16* w4=(const u16*)d_in[13];
  char* ws=(char*)d_ws; size_t MB=1<<20;
  u16* xn=(u16*)(ws); u16* xn2=(u16*)(ws+2*MB); u16* h1=(u16*)(ws+4*MB); u16* hs=(u16*)(ws+6*MB); u16* x1=(u16*)(ws+8*MB); u16* x1n=(u16*)(ws+10*MB);
  u16* h3=(u16*)(ws+12*MB); float* t0=(float*)(ws+20*MB); float* t1=(float*)(ws+40*MB); float* hre=(float*)(ws+48*MB); float* him=(float*)(ws+56*MB); float* y=(float*)(ws+64*MB);
  ln_k<<<S,64,0,stream>>>(x,xn); ln_k<<<S,64,0,stream>>>(xn,xn2);
  gemm_k<1><<<dim3(P/64,S),64,0,stream>>>(xn,bre,t0,P,D); gemm_k<1><<<dim3(P/64,S),64,0,stream>>>(xn,bim,t1,P,D);
  scan_k<<<P/64,64,0,stream>>>(t0,t1,lre,lim,ldt,hre,him);
  yc_k<<<dim3(D/64,S),64,0,stream>>>(hre,him,cre,cim,y);
  gemm_k<0><<<dim3(D/64,S),64,0,stream>>>(xn2,w1,t0,D,D); gelu_k<<<S*D/256,256,0,stream>>>(t0,h1,S*D);
  shift_k<<<S*D/256,256,0,stream>>>(h1,hs);
  gemm_k<0><<<dim3(D/64,S),64,0,stream>>>(hs,w2,t0,D,D); addres_k<<<S*D/256,256,0,stream>>>(xn,t0,x1,S*D);
  ln_k<<<S,64,0,stream>>>(x1,x1n);
  gemm_k<0><<<dim3(F/64,S),64,0,stream>>>(x1n,w3,t0,F,D); gelu_k<<<S*F/256,256,0,stream>>>(t0,h3,S*F);
  gemm_k<0><<<dim3(D/64,S),64,0,stream>>>(h3,w4,t1,D,F);
  final_k<<<S*D/256,256,0,stream>>>(x,xn,dsk,y,x1,t1,(float*)d_out);
}
```

```cpp
#include <hip/hip_runtime.h>
#include <hip/hip_bf16.h>
#include <stdint.h>

namespace s5k {
typedef unsigned short u16;
typedef unsigned int u32;
typedef unsigned long long u64;
typedef __attribute__((ext_vector_type(4))) short s16x4;
typedef __attribute__((ext_vector_type(8))) short s16x8;
typedef __attribute__((ext_vector_type(2))) __bf16 bf16x2;
typedef __attribute__((ext_vector_type(8))) __bf16 bf16x8;
typedef __attribute__((ext_vector_type(2))) float f32x2;
typedef __attribute__((ext_vector_type(4))) float f32x4;
typedef __attribute__((ext_vector_type(16))) float f32x16;
typedef __attribute__((ext_vector_type(2))) u32 u32x2;
typedef __attribute__((ext_vector_type(4))) u32 u32x4;
#define LAS __attribute__((address_space(3)))
#define GAS __attribute__((address_space(1)))
#define DI __device__ __forceinline__
typedef __amdgpu_buffer_rsrc_t rsrc_t;

constexpr int S = 2048, D = 512, F = 1792;
constexpr float EPS = 1e-5f;
constexpr int NTHR = 768;
constexpr int SLOT = 8192, GRP = 16384, NWG = 5;
constexpr int LDS_A = 0, LDS_W = 4 * GRP, LDS_S = LDS_W + NWG * GRP, LDS_BYTES = 163840;
constexpr int SC_TAG = LDS_S;
constexpr int SC_STAT = LDS_S + 256;
constexpr int SC_E1 = LDS_S + 768;
constexpr int SC_CS3 = LDS_S + 1280;
constexpr int SC_STAT3 = LDS_S + 2304;
constexpr int SC_LAM = LDS_S + 3584;
constexpr int SC_LG = LDS_S + 6656;
constexpr int SC_CSB = LDS_S + 8704;
constexpr int SC_HIN = LDS_S + 9216;
constexpr int WS_EPOCH = 0;
constexpr int WS_FLAG = 4096;
constexpr int FL_H1 = 0, FL_X1 = 1, FL_H = 2, FL_H3 = 3;
constexpr int WS_E = WS_FLAG + 4 * 256 * 128;
constexpr int WS_CSB = WS_E + 32 * 512 * 16;
constexpr int WS_CS3 = WS_CSB + 512 * 16;
constexpr int WS_H1 = WS_CS3 + 56 * 8 * 256;
constexpr int WS_X1 = WS_H1 + 32 * 65536;
constexpr int WS_H = WS_X1 + 32 * 65536;
constexpr int WS_H3 = WS_H + 32 * 131072;
constexpr int WS_END = WS_H3 + 32 * 229376;
constexpr int WS_STAMP = WS_END;
constexpr int OOB = 0x40000000;
constexpr int SC1 = 16;
constexpr u32 SPIN_CAP = 1u << 16;

struct Params {
  const u16 *x, *lre, *lim, *bre, *bim, *cre, *cim, *ldt, *dsk, *w1, *w2, *w3, *w4;
  float* out;
  char* ws;
};

DI rsrc_t mkrsrc(const void* p, int bytes) { return __builtin_amdgcn_make_buffer_rsrc((void*)p, 0, bytes, 0x00020000); }
DI float bf2f(u32 v) { return __uint_as_float(v << 16); }
DI float bflo(u32 v) { return __uint_as_float(v << 16); }
DI float bfhi(u32 v) { return __uint_as_float(v & 0xffff0000u); }
DI u32 pack2(float a, float b) { f32x2 v = {a, b}; return __builtin_bit_cast(u32, __builtin_convertvector(v, bf16x2)); }
DI float dot2(u32 a, u32 b, float c) {
  return __builtin_amdgcn_fdot2_f32_bf16(__builtin_bit_cast(bf16x2, a), __builtin_bit_cast(bf16x2, b), c, false);
}
constexpr u32 ONES = 0x3f803f80u;
DI float sum8(u32x4 v, float c) { c = dot2(v[0], ONES, c); c = dot2(v[1], ONES, c); c = dot2(v[2], ONES, c); return dot2(v[3], ONES, c); }
DI float ssq8(u32x4 v, float c) { c = dot2(v[0], v[0], c); c = dot2(v[1], v[1], c); c = dot2(v[2], v[2], c); return dot2(v[3], v[3], c); }
DI float xsum32(float x) { auto r = __builtin_amdgcn_permlane32_swap(__float_as_uint(x), __float_as_uint(x), false, false); return __uint_as_float(r[0]) + __uint_as_float(r[1]); }
DI float xsum16(float x) { auto r = __builtin_amdgcn_permlane16_swap(__float_as_uint(x), __float_as_uint(x), false, false); return __uint_as_float(r[0]) + __uint_as_float(r[1]); }
DI f32x4 mfma16(u32x4 a, u32x4 b, f32x4 c) {
  return __builtin_amdgcn_mfma_f32_16x16x32_bf16(__builtin_bit_cast(bf16x8, a), __builtin_bit_cast(bf16x8, b), c, 0, 0, 0);
}
DI f32x16 mfma32(u32x4 a, u32x4 b, f32x16 c) {
  return __builtin_amdgcn_mfma_f32_32x32x16_bf16(__builtin_bit_cast(bf16x8, a), __builtin_bit_cast(bf16x8, b), c, 0, 0, 0);
}
DI u32x4 lds128(u32 a) { return *(const LAS u32x4*)(a); }
DI u32x2 ldstr(u32 a) { return __builtin_bit_cast(u32x2, __builtin_amdgcn_ds_read_tr16_b64_v4i16((LAS s16x4*)(a))); }
DI u32x2 lds64(u32 a) { return *(const LAS u32x2*)(a); }
DI u32 lds16(u32 a) { return *(const LAS u16*)(a); }
DI u32x4 ldstr2(u32 a0, u32 a1) { u32x2 x = ldstr(a0), y = ldstr(a1); return u32x4{x[0], x[1], y[0], y[1]}; }
DI void bar() {
  asm volatile("" ::: "memory");
  __builtin_amdgcn_s_waitcnt(0xC07F);
  asm volatile("s_barrier" ::: "memory");
}
DI void bard() {
  asm volatile("" ::: "memory");
  __builtin_amdgcn_s_waitcnt(0x0070);
  asm volatile("s_waitcnt vmcnt(0)\n\ts_barrier" ::: "memory");
}
DI void lbar() { asm volatile("s_barrier" ::: "memory"); }
DI void st128(u32x4 v, rsrc_t rs, int off) { __builtin_amdgcn_raw_buffer_store_b128(v, rs, off, 0, SC1); }
DI void drain() { asm volatile("s_waitcnt vmcnt(0)" ::: "memory"); }
constexpr float GC0 = -2.0f * 0.7978845608028654f * 1.4426950408889634f, GC1 = GC0 * 0.044715f;
DI u32 gelu2(f32x2 a, float k0, float k1, float ri) {
  f32x2 t = a * a;
  t = __builtin_elementwise_fma(t, f32x2{k1, k1}, f32x2{k0, k0}) * a;
  f32x2 e = {__builtin_amdgcn_exp2f(t[0]), __builtin_amdgcn_exp2f(t[1])};
  f32x2 d = __builtin_elementwise_fma(e, f32x2{ri, ri}, f32x2{ri, ri});
  f32x2 q = {__builtin_amdgcn_rcpf(d[0]), __builtin_amdgcn_rcpf(d[1])};
  return __builtin_bit_cast(u32, __builtin_convertvector(a * q, bf16x2));
}
DI int swx(int t) { return ((t >> 1) & 1) | ((t >> 2) & 2) | ((t ^ (t >> 2)) & 4); }
typedef f32x2 cf;
DI cf cmad(cf a, cf b, cf c) {
  cf t = __builtin_elementwise_fma(cf{a[0], a[0]}, b, c);
  return __builtin_elementwise_fma(cf{-a[1], a[1]}, cf{b[1], b[0]}, t);
}
DI cf cmul(cf a, cf b) { return __builtin_elementwise_fma(cf{-a[1], a[1]}, cf{b[1], b[0]}, cf{a[0], a[0]} * b); }
DI cf cpow(float la, float lb, float n) {
  float m = __builtin_amdgcn_exp2f(n * la), f = __builtin_amdgcn_fractf(n * lb);
  return cf{m * __builtin_amdgcn_cosf(f), m * __builtin_amdgcn_sinf(f)};
}
#define PIN1(a) asm volatile("" : "+v"(a)::"memory")
#define PIN2(a, b) asm volatile("" : "+v"(a), "+v"(b)::"memory")
#define PIN3(a, b, c) asm volatile("" : "+v"(a), "+v"(b), "+v"(c)::"memory")
#define PIN4(a, b, c, d) asm volatile("" : "+v"(a), "+v"(b), "+v"(c), "+v"(d)::"memory")
#define PIN6(a, b, c, d, e, f) asm volatile("" : "+v"(a), "+v"(b), "+v"(c), "+v"(d), "+v"(e), "+v"(f)::"memory")
DI u64 ld_u64(const char* p) { return __hip_atomic_load((const GAS u64*)p, __ATOMIC_RELAXED, __HIP_MEMORY_SCOPE_AGENT); }
DI void st_u64(char* p, u64 v) { __hip_atomic_store((GAS u64*)p, v, __ATOMIC_RELAXED, __HIP_MEMORY_SCOPE_AGENT); }
DI u64 make_tag(u64 e) {
  u64 z = e + 0x9E3779B97F4A7C15ull;
  z = (z ^ (z >> 30)) * 0xBF58476D1CE4E5B9ull;
  z = (z ^ (z >> 27)) * 0x94D049BB133111EBull;
  z ^= z >> 31;
  u32 lo = (u32)z, hi = (u32)(z >> 32);
  lo |= 1u; hi |= 1u;
  if (lo == (u32)e) lo ^= 0x5bd1e994u;
  if (hi == (u32)(e >> 32)) hi ^= 0x5bd1e994u;
  return ((u64)hi << 32) | lo;
}
#define STAMPSRC __builtin_amdgcn_s_memrealtime
#define STAMP_DECL
#define STAMP(k) asm volatile("; MARK " #k)
#define STAMP_OUT()
#define STAMP_ARG
#define STAMP_PASS

template <int V> struct IC { static constexpr int v = V; };
template <int I, int N, class Fn> DI void sfor(Fn&& f) {
  if constexpr (I < N) { f(IC<I>{}); sfor<I + 1, N>(f); }
}
template <int N> DI void wait_vm() { asm volatile("s_waitcnt vmcnt(%0)" ::"n"(N) : "memory"); }
template <int AUX> DI void dma4(rsrc_t rs, u32 lds, int v0, int v1, int v2, int v3, int so) {
  __builtin_amdgcn_raw_ptr_buffer_load_lds(rs, (LAS void*)lds, 16, v0, so, 0, AUX);
  __builtin_amdgcn_raw_ptr_buffer_load_lds(rs, (LAS void*)lds, 16, v1, so, 1024, AUX);
  __builtin_amdgcn_raw_ptr_buffer_load_lds(rs, (LAS void*)lds, 16, v2, so, 2048, AUX);
  __builtin_amdgcn_raw_ptr_buffer_load_lds(rs, (LAS void*)lds, 16, v3, so, 3072, AUX);
}
constexpr int P_G1 = 0, P_GB = 4, P_G2 = 12, P_R = 16, P_G3 = 24, P_GC = 32, P_XC = 40, P_G4 = 41, P_X4 = 55;
constexpr int T_G1 = 0, T_GB = 5, T_E0 = 13, T_G2 = 15, T_RA = 19, T_X1 = 20, T_RB = 21, T_RC = 22, T_G3 = 23, T_GC = 31, T_XC = 39,
              T_G4 = 40, T_END = 54;
constexpr int cmin(int a, int b) { return a < b ? a : b; }
constexpr int w_need(int t) {
  return t < T_GB ? cmin(t + 1, 4) : t < T_G2 ? P_GB + cmin(t - T_GB + 1, 8) : t < T_RA ? P_G2 + t - T_G2 + 1 : t < T_RB ? P_R + 3
       : t < T_G3 ? P_G3 : t < T_XC ? P_G3 + t - T_G3 + 1 : t < T_G4 ? P_XC + 1 : P_G4 + t - T_G4 + 1;
}
constexpr int w_issued(int t) {
  return t < 0 ? 2 : t == 0 ? 3 : t == 1 ? 5 : t == T_RC ? P_G3 + 3 : cmin(P_X4, NWG + w_need(t - 1));
}
constexpr int w_dma(int a, int b) { return 8 * (b - a - (a <= P_XC && P_XC < b ? 1 : 0)); }
constexpr u32 wgr(int s) { return (s % NWG) * GRP; }

DI void weight_loader(const Params& p, int wl, int lane, int j) {
  __builtin_amdgcn_s_setprio(2);
  const u32 lo = LDS_W + wl * 4096;
  int v1[4], vb[4], v2[4], v3[4], v4[4];
#pragma unroll
  for (int i = 0; i < 4; ++i)
    v1[i] = (32 * wl + 7 * i + (lane >> 3)) * 1024 + (((lane & 7) ^ ((((lane >> 4) & 1) | ((i & 1) << 1)) << 1)) << 4);
  const rsrc_t r1 = mkrsrc(p.w1, D * D * 2);
  const u16 *pw2 = p.w2, *pw3 = p.w3, *pw4 = p.w4, *pcr = p.cre, *pci = p.cim, *pbr = p.bre, *pbi = p.bim;
  auto issue = [&](auto N) {
    constexpr int n = N.v;
    const u32 dst = lo + wgr(n);
    auto two = [&](rsrc_t ra, rsrc_t rb, const int* v, int so, int d1) {
      dma4<0>(ra, dst, v[0], v[1], v[2], v[3], so);
      dma4<0>(rb, dst + SLOT, v[0], v[1], v[2], v[3], so + d1);
    };
    if constexpr (n < P_GB) two(r1, r1, v1, 128 * j + n * 131072, 65536);
    else if constexpr (n < P_G2) two(mkrsrc(pbr, D * D * 2), mkrsrc(pbi, D * D * 2), vb, 65536 * j + (n - P_GB) * 128, 0);
    else if constexpr (n < P_R) two(mkrsrc(pw2, D * D * 2), mkrsrc(pw2, D * D * 2), v2, 128 * j + (n - P_G2) * 131072, 65536);
    else if constexpr (n < P_GC)
      two(mkrsrc(pw3, D * F * 2), mkrsrc(pw3, D * F * 2), v3, 448 * j + (64 * ((n - P_R) & 7) + 32 * ((n - P_R) >> 3)) * (F * 2), 8 * F * 2);
    else if constexpr (n < P_XC) two(mkrsrc(pcr, D * D * 2), mkrsrc(pci, D * D * 2), vb, 65536 * j + (n - P_GC) * 128, 0);
    else if constexpr (n > P_XC) two(mkrsrc(pw4, D * F * 2), mkrsrc(pw4, D * F * 2), v4, 128 * j + (n - P_G4) * 131072, 65536);
  };
  sfor<0, w_issued(-1)>(issue);
  {
    int ln = lane;
    asm volatile("" : "+v"(ln)::"memory");
    asm volatile("" : "+s"(pw2), "+s"(pw3), "+s"(pw4), "+s"(pcr), "+s"(pci), "+s"(pbr), "+s"(pbi)::"memory");
    const int r = ln >> 3, cc = ln & 7;
#pragma unroll
    for (int i = 0; i < 4; ++i) {
      vb[i] = (32 * wl + 7 * i + r) * 1024 + ((cc ^ (((ln >> 4) + 4 * i) & 7)) << 4);
      v2[i] = (32 * wl + 16 * (ln >> 5) + 3 * i + (r & 3)) * 1024 + ((cc ^ ((((ln >> 4) & 1) | ((i & 1) << 1)) << 1)) << 4);
      v4[i] = (32 * wl + 7 * i + r) * 1024 + ((cc ^ (((ln >> 4) & 1) << 2)) << 4);
      int ch = (4 * wl + i) * 64 + ln, rr = ch / 28, c3 = ch - rr * 28;
      v3[i] = ch < 448 ? (16 * ((rr >> 2) & 1) + 4 * (rr >> 3) + (rr & 3)) * (F * 2) + c3 * 16 - i * 1024 : OOB;
    }
    PIN4(vb[0], vb[1], vb[2], vb[3]); PIN4(v2[0], v2[1], v2[2], v2[3]); PIN4(v3[0], v3[1], v3[2], v3[3]); PIN4(v4[0], v4[1], v4[2], v4[3]);
  }
  sfor<0, T_END>([&](auto T) {
    constexpr int t = T.v;
    if constexpr (t == 0 || w_need(t) != w_need(t - 1)) wait_vm<w_dma(w_need(t), w_issued(t - 1))>();
    lbar();
    sfor<w_issued(t - 1), w_issued(t)>(issue);
  });
}

DI u64 peek(const char* p) { u64 v; asm volatile("global_load_dwordx2 %0, %1, off sc1" : "=v"(v) : "v"(p) : "memory"); return v; }
template <int N> DI void settle(u64& v) { asm volatile("s_waitcnt vmcnt(%1)" : "+v"(v) : "n"(N) : "memory"); }
DI void spin(const char* p, bool use, u64 v, u64 tag) {
#pragma unroll 1
  for (u32 sp = 0; sp < SPIN_CAP && !__all(!use || v == tag); ++sp) { v = peek(p); settle<0>(v); }
}
constexpr int T_H = T_G3 + 5, T_P3 = T_GC + 5, T_H3 = T_GC + 7;
constexpr int a_issued(int t) {
  return t < T_H ? 0 : t < T_GC ? (t - T_G3) / 2 : t < T_H3 ? cmin(8, 4 + t - T_GC) : t <= T_XC ? 4 + t - T_GC : cmin(22, 12 + t - T_G4);
}
DI void act_loader(const Params& p, int al, int lane, int b, int c, int j STAMP_ARG) {
  __builtin_amdgcn_s_setprio(3);
  const u32 lo = LDS_A + al * 4096;
  const rsrc_t rx = mkrsrc(p.x, S * D * 2);
  int vo[4];
#pragma unroll
  for (int i = 0; i < 4; ++i) { int t = 8 * i + (lane >> 3); vo[i] = (32 * al + t - i) * 1024 + (((lane & 7) ^ swx(t)) << 4); }
  auto ldx = [&](int m) { dma4<0>(rx, lo + m * SLOT, vo[0], vo[1], vo[2], vo[3], c * 65536 + m * 128); };
  ldx(0); ldx(1); ldx(2); ldx(3);
  char* const ws = p.ws;
  const rsrc_t rw = mkrsrc(ws, WS_END), re_ = mkrsrc(ws + WS_E, 32 * 512 * 16);
  int vl = al * 4096 + lane * 16, vh[4];
#pragma unroll
  for (int m = 0; m < 4; ++m) {
    int t = 64 * c + lane + ((m & 1) ? (m >> 1) + 1 : -((m >> 1) + 1));
    vh[m] = (t >= 0 && t < S) ? (t >> 6) * 65536 + (t & 63) * 16 + al * 4096 : OOB;
  }
  const int cn = lane < 8 ? c : c + (((lane >> 1) & 1) ? 1 : -1);
  const bool use1 = lane < 16 && cn >= 0 && cn < 32, use0 = lane < 8;
  const char* f1 = ws + WS_FLAG + ((use1 ? cn : c) * 8 + (lane & 7)) * 128;
  const char* f0 = ws + WS_FLAG + (c * 8 + (lane & 7)) * 128;
  char* const fme = ws + WS_FLAG + (c * 8 + j) * 128;
  PIN4(vh[0], vh[1], vh[2], vh[3]); PIN3(vl, f1, f0);
  const bool pub = al == 0 && lane == 0;
  const int rg = b / 28, cb = b - 28 * rg, i16 = lane & 15, g = lane >> 4;
  auto ldh1 = [&](int m) {
    dma4<SC1>(rw, lo + m * GRP, vh[m], vh[m], vh[m], vh[m], WS_H1 + m * GRP);
    dma4<SC1>(rw, lo + m * GRP + SLOT, vh[m], vh[m], vh[m], vh[m], WS_H1 + m * GRP + SLOT);
  };
  auto ldx1 = [&](int m) { dma4<SC1>(rw, lo + m * SLOT, vl, vl, vl, vl, WS_X1 + c * 65536 + m * SLOT); };
  u64 tag = 0, pk = 0;
  u32 tlo = 0, thi = 0;
  u32x2 epv;
  u32 q_lr, q_li, q_dt;
  u32x4 jb[16];
  cf l64, lfin;
  const int c3o = WS_CS3 + (7 * j + 4 * al) * 2048 + lane * 16;
  auto ld_cs = [&]() {
#pragma unroll
    for (int i = 0; i < 8; ++i) jb[i] = __builtin_amdgcn_raw_buffer_load_b128(rw, (al && i >= 6) ? WS_CSB + (64 * j + lane) * 16 : c3o + i * 1024, 0, SC1);
  };
  const int evo = (64 * j + lane) * 16 + (c - 1 - 16 * al) * 8192;
  auto ld_e = [&]() {
#pragma unroll
    for (int d = 0; d < 16; ++d) jb[d] = __builtin_amdgcn_raw_buffer_load_b128(re_, evo - d * 8192, 0, SC1);
  };
  sfor<0, T_END>([&](auto T) {
    constexpr int t = T.v;
    constexpr int s = t >= T_G4 ? t - T_G4 + 8 : t - T_GC;
    if constexpr (t < 2) wait_vm<8>();
    if constexpr (t == 2 || t == 3) wait_vm<8 * (3 - t) + 8>();
    if constexpr (t == T_G2) wait_vm<16>();
    if constexpr (t > T_G2 && t < T_G2 + 4) wait_vm<8 * (3 - (t - T_G2))>();
    if constexpr (t == T_G3) wait_vm<8>();
    if constexpr (t == T_G3 + 1) wait_vm<12>();
    if constexpr (t == T_G3 + 2 || t == T_G3 + 3) wait_vm<4 * (6 - (t - T_G3))>();
    if constexpr (t == T_G3 + 4) settle<0>(pk);
    if constexpr ((t >= T_GC && t < T_XC) || t >= T_G4) wait_vm<8 * (a_issued(t - 1) - s - 1)>();
    lbar();
    if constexpr (t == 0) { STAMP(5); ldx(4); ldx(5); }
    if constexpr (t == 1) {
      ldx(6); ldx(7);
      asm volatile("" ::: "memory");
      epv = __builtin_amdgcn_raw_buffer_load_b64(rw, WS_EPOCH, 0, SC1);
      q_lr = p.lre[64 * j + lane]; q_li = p.lim[64 * j + lane]; q_dt = p.ldt[64 * j + lane];
      if (al == 0) {
        jb[0] = *(const u32x4*)(p.bre + 2 * b * D + lane * 8); jb[1] = *(const u32x4*)(p.bim + 2 * b * D + lane * 8);
        jb[2] = *(const u32x4*)(p.bre + (2 * b + 1) * D + lane * 8); jb[3] = *(const u32x4*)(p.bim + (2 * b + 1) * D + lane * 8);
      } else {
        const rsrc_t r3 = mkrsrc(p.w3, b < 224 ? D * F * 2 : 0);
#pragma unroll
        for (int i = 0; i < 8; ++i) jb[i] = __builtin_amdgcn_raw_buffer_load_b128(r3, (lane >> 3) * (F * 2) + (lane & 7) * 16, (64 * rg + 8 * i) * (F * 2) + cb * 128, 0);
      }
    }
    if constexpr (t == 3) {
      asm volatile("" : "+v"(epv));
      tag = make_tag(((u64)(u32)__builtin_amdgcn_readfirstlane(epv[1]) << 32) | (u32)__builtin_amdgcn_readfirstlane(epv[0]));
      tlo = (u32)tag; thi = (u32)(tag >> 32);
      f32x4 z = {0, 0, 0, 0};
      if (al == 0) {
        const u32x4 ones = {ONES, ONES, ONES, ONES};
        float v[4];
#pragma unroll
        for (int i = 0; i < 4; ++i) { f32x4 sr = mfma16(jb[i], ones, z); v[i] = xsum32(xsum16(sr[0] + sr[1] + sr[2] + sr[3])); }
        st128(u32x4{__float_as_uint(lane ? v[2] : v[0]), tlo, __float_as_uint(lane ? v[3] : v[1]), thi}, rw, lane < 2 ? WS_CSB + (2 * b + lane) * 16 : OOB);
        if (lane == 0) *(LAS u64*)(SC_TAG) = tag;
      } else {
        const u32 one = i16 < 8 ? 0x3f80u << (16 * (i16 & 1)) : 0u;
        const u32x4 sel = {(i16 >> 1) == 0 ? one : 0u, (i16 >> 1) == 1 ? one : 0u, (i16 >> 1) == 2 ? one : 0u, (i16 >> 1) == 3 ? one : 0u};
#pragma unroll
        for (int i = 0; i < 8; ++i) z = mfma16(jb[i], sel, z);
        const int o = b < 224 && i16 < 8 && g < 2 ? WS_CS3 + ((2 * cb + g) * 8 + rg) * 256 + i16 * 16 : OOB;
        st128(u32x4{__float_as_uint(xsum32(z[0])), tlo, __float_as_uint(xsum32(z[1])), thi}, rw, o);
        st128(u32x4{__float_as_uint(xsum32(z[2])), tlo, __float_as_uint(xsum32(z[3])), thi}, rw, o + 128);
      }
    }
    if constexpr (t == 4) {
      asm volatile("" : "+v"(q_lr), "+v"(q_li), "+v"(q_dt));
      float lr = bf2f(q_lr), li = bf2f(q_li);
      float dt = bflo(pack2(__builtin_amdgcn_exp2f(bf2f(q_dt) * 1.4426950408889634f), 0.f));
      float la = dt * lr * 1.4426950408889634f, lb = dt * li * 0.15915494309189535f;
      l64 = cpow(la, lb, 64.f);
      cf l8 = cpow(la, lb, 8.f);
      if (al == 0) {
        cf lam = cpow(la, lb, 1.f), l32 = cpow(la, lb, 32.f);
        float nr = lam[0] - 1.0f, ni = lam[1], id = __builtin_amdgcn_rcpf(lr * lr + li * li);
        LAS f32x4* tp = (LAS f32x4*)(SC_LAM + lane * 48);
        tp[0] = f32x4{lam[0], lam[1], (nr * lr + ni * li) * id, (ni * lr - nr * li) * id};
        tp[1] = f32x4{l8[0], l8[1], l32[0], l32[1]};
        lfin = cf{1.f, 0.f};
      } else {
        LAS f32x4* tp = (LAS f32x4*)(SC_LG + lane * 32);
        cf l16 = cmul(l8, l8), l24 = cmul(l16, l8);
        tp[0] = f32x4{1.f, 0.f, l8[0], l8[1]};
        tp[1] = f32x4{l16[0], l16[1], l24[0], l24[1]};
        lfin = cpow(la, lb, 1024.f);
      }
      asm volatile("s_waitcnt lgkmcnt(0)" ::: "memory");
    }
    if constexpr (t == T_GB + 2) { if (pub) st_u64(fme + FL_H1 * 32768, tag); }
    if constexpr (t == T_GB + 3) ld_cs();
    if constexpr (t == T_E0 - 2) pk = peek(f1 + FL_H1 * 32768);
    if constexpr (t == T_E0 - 1) {
#pragma unroll 1
      for (u32 sp = 0; sp < SPIN_CAP; ++sp) {
        u32 bad = 0;
#pragma unroll
        for (int i = 0; i < 8; ++i) bad |= (jb[i][1] ^ tlo) | (jb[i][3] ^ thi);
        if (__all(bad == 0)) break;
        ld_cs();
      }
      if (al) *(LAS f32x2*)(SC_CSB + lane * 8) = f32x2{__uint_as_float(jb[6][0]), __uint_as_float(jb[6][2])};
      asm volatile("s_waitcnt lgkmcnt(0)" ::: "memory");
    }
    if constexpr (t == T_E0) {
      settle<0>(pk); spin(f1 + FL_H1 * 32768, use1, pk, tag);
      STAMP(1);
      ldh1(0); ldh1(1);
#pragma unroll
      for (int q = 0; q < 4; ++q) {
        float u0 = xsum32(xsum16(__uint_as_float(jb[2 * q][0]) + __uint_as_float(jb[2 * q + 1][0])));
        float u1 = xsum32(xsum16(__uint_as_float(jb[2 * q][2]) + __uint_as_float(jb[2 * q + 1][2])));
        const u32 ca = SC_CS3 + (4 * al + q) * 128 + (16 * (i16 >> 3) + (i16 & 7)) * 4;
        asm volatile("ds_write2_b32 %0, %1, %2 offset1:8" ::"v"(ca), "v"(u0), "v"(u1) : "memory");
      }
    }
    if constexpr (t == T_E0 + 1) ldh1(2);
    if constexpr (t == T_G2) ldh1(3);
    if constexpr (t == T_G2 + 3) ld_e();
    if constexpr (t == T_X1) {
      if (pub) st_u64(fme + FL_X1 * 32768, tag);
      const int nv = min(max(c - 16 * al, 0), 16);
#pragma unroll 1
      for (u32 sp = 0; sp < SPIN_CAP; ++sp) {
        int cnt = 0;
#pragma unroll
        for (int d = 0; d < 16; ++d) cnt += ((jb[d][1] ^ tlo) | (jb[d][3] ^ thi)) == 0;
        if (__all(cnt == nv)) break;
        ld_e();
      }
      cf part = {__uint_as_float(jb[15][0]), __uint_as_float(jb[15][2])};
#pragma unroll
      for (int d = 14; d >= 0; --d) part = cmad(l64, part, cf{__uint_as_float(jb[d][0]), __uint_as_float(jb[d][2])});
      *(LAS f32x2*)(SC_HIN + lane * 16 + al * 8) = cmul(lfin, part);
      asm volatile("s_waitcnt lgkmcnt(0)" ::: "memory");
      STAMP(6);
    }
    if constexpr (t == T_RB) pk = peek(f0 + FL_X1 * 32768);
    if constexpr (t == T_RC) {
      settle<0>(pk); spin(f0 + FL_X1 * 32768, use0, pk, tag);
      STAMP(2);
      ldx1(0); ldx1(1); ldx1(2); ldx1(3);
    }
    if constexpr (t == T_G3) { ldx1(4); ldx1(5); }
    if constexpr (t == T_G3 + 1) { ldx1(6); ldx1(7); if (pub) st_u64(fme + FL_H * 32768, tag); }
    if constexpr (t == T_G3 + 3) pk = peek(f0 + FL_H * 32768);
    if constexpr (t == T_H) { spin(f0 + FL_H * 32768, use0, pk, tag); STAMP(3); }
    if constexpr (t == T_P3) { if (pub) st_u64(fme + FL_H3 * 32768, tag); }
    if constexpr (t == T_H3) { spin(f0 + FL_H3 * 32768, use0, ~tag, tag); STAMP(4); }
    sfor<a_issued(t - 1), a_issued(t)>([&](auto Q) {
      constexpr int q = Q.v;
      const int so = q < 8 ? WS_H + c * 131072 + q * GRP : WS_H3 + c * 229376 + (q - 8) * GRP;
      dma4<SC1>(rw, lo + (q & 3) * GRP, vl, vl, vl, vl, so);
      dma4<SC1>(rw, lo + (q & 3) * GRP + SLOT, vl, vl, vl, vl, so + SLOT);
    });
  });
}

#define WADR(lo_, s) ((s) % NWG == 4 ? lo_##h : lo_ + wgr(s))
#define LANEVARS(ln)                                                                                                 \
  int ln = lane; asm volatile("" : "+v"(ln)::"memory");                                                              \
  const int i16 = ln & 15, g = ln >> 4, l31 = ln & 31, hh = ln >> 5, qq = (ln & 15) >> 2, pp = ln & 3, blk = g & 1;  \
  const int tok1 = 16 * tq + i16, tf0 = 32 * th4 + 16 * kh4 + 4 * hh, chn = 64 * j + 16 * cg + i16;                  \
  (void)l31; (void)qq; (void)pp; (void)blk; (void)tok1; (void)tf0; (void)chn

__global__ __launch_bounds__(NTHR, 3) void fused_kernel(Params p) {
  extern __shared__ __attribute__((aligned(16))) char smem[];
  const int lane = threadIdx.x & 63;
  const int w = __builtin_amdgcn_readfirstlane(threadIdx.x >> 6);
  const int b = blockIdx.x;
  const int c = (b & 7) * 4 + (b >> 6), j = (b >> 3) & 7;
  STAMP_DECL;
  STAMP(0);
  if (w >= 10) { act_loader(p, w - 10, lane, b, c, j STAMP_PASS); STAMP(15); STAMP_OUT(); return; }
  if (w >= 8) { weight_loader(p, w - 8, lane, j); STAMP(15); STAMP_OUT(); return; }

  const int cg = w & 3, tb = w >> 2;
  const int nh4 = (w >> 1) & 1, th4 = w & 1, kh4 = w >> 2;
  const int nh = w & 1, tq = w >> 1;
  const rsrc_t rw = mkrsrc(p.ws, WS_END);
  const u32x4 ones = {ONES, ONES, ONES, ONES};
  const u32 q_dk = p.dsk[64 * j + 32 * nh4 + (lane & 31)];
  u32 touch;
  {
    const u16* src = w == 0 ? p.w4 : w == 1 ? p.w2 : w == 2 ? p.cre : p.cim;
    const int nl = w == 0 ? 56 : 16;
    touch = __builtin_amdgcn_raw_buffer_load_b32(mkrsrc(src, nl * 32768), lane < nl && w < 4 ? (b * nl + lane) * 128 : OOB, 0, 0);
  }
  u32 wo0, wo1, a1o, a1x, ab0, ab1, bbo, bbx, bboh, bbxh, xra, xfa[4], a2o, sta, stb, lta, lga;
  int so12, cso;
  bool dia, m1, m2, m3;
  {
    LANEVARS(ln);
    wo0 = LDS_W + (8 * g + qq) * 128 + (((2 * nh) ^ ((qq >> 1) | (blk << 1))) << 5) + 8 * pp; wo1 = wo0 ^ 32;
    a1o = tok1 * 128 + ((g ^ swx(tok1)) << 4); a1x = a1o ^ 64;
    so12 = c * 65536 + j * 8192 + (4 * nh + g) * 1024 + tok1 * 16;
    const int t0 = 32 * tb + 8 * qq + pp, n = 16 * cg + i16;
    ab0 = t0 * 128 + ((g ^ swx(t0)) << 4); ab1 = ab0 ^ 64;
    bbo = LDS_W + n * 128 + ((g ^ ((n >> 1) & 7)) << 4); bbx = bbo ^ 64; bboh = bbo + 4 * GRP; bbxh = bbx + 4 * GRP;
    xra = j * SLOT + tok1 * 128 + (((4 * nh + (g >> 1)) ^ swx(tok1)) << 4) + 8 * (g & 1);
#pragma unroll
    for (int r = 0; r < 4; ++r) {
      int t = tf0 + 8 * (r >> 1) + 2 * (r & 1);
      xfa[r] = j * SLOT + t * 128 + (((4 * nh4 + (l31 >> 3)) ^ swx(t)) << 4) + (l31 & 7) * 2;
    }
    a2o = g * 1024 + tok1 * 16;
    sta = SC_STAT + tok1 * 8; stb = SC_STAT + (32 * tb + 8 * g) * 8;
    lta = SC_LAM + n * 48; lga = SC_LG + n * 32 + g * 8;
    cso = SC_CSB + n * 8;
    dia = g == qq; m1 = pp == 1; m2 = pp == 2; m3 = pp == 3;
    PIN6(wo0, wo1, a1o, a1x, so12, ab0); PIN6(ab1, bbo, bbx, xra, xfa[0], xfa[1]); PIN6(xfa[2], xfa[3], a2o, bboh, bbxh, sta);
    PIN4(stb, lta, lga, cso);
  }
  STAMP(14);

  float mean1, rstd1;
  {
    f32x4 a0 = {0, 0, 0, 0}, a1 = a0, c0 = a0, c1 = a0, sm = a0, gr = a0;
    u32x4 X[4], F0[4], F1[4];
    auto ld = [&](int q, int s) {
      X[q] = lds128(LDS_A + s * GRP + (q >> 1) * SLOT + ((q & 1) ? a1x : a1o));
      F0[q] = ldstr2(wo0 + wgr(s) + q * 4096, wo0 + wgr(s) + q * 4096 + 512);
      F1[q] = ldstr2(wo1 + wgr(s) + q * 4096, wo1 + wgr(s) + q * 4096 + 512);
    };
    auto mm = [&](int q, bool first) {
      if (first) asm volatile("" ::: "memory"); else PIN6(a0, a1, c0, c1, sm, gr);
      sm = mfma16(ones, X[q], sm); gr = mfma16(X[q], X[q], gr);
      a0 = mfma16(F0[q], X[q], a0); c0 = mfma16(F0[q], ones, c0);
      a1 = mfma16(F1[q], X[q], a1); c1 = mfma16(F1[q], ones, c1);
      PIN6(a0, a1, c0, c1, sm, gr);
    };
    sfor<0, 4>([&](auto SS) {
      constexpr int s = SS.v;
      bar();
      ld(0, s); ld(1, s);
      if (s) mm(2, false);
      ld(2, s);
      if (s) mm(3, false);
      ld(3, s);
      mm(0, s == 0); mm(1, false);
    });
    bar(); mm(2, false); mm(3, false);
    STAMP(1);
    float dg = m3 ? gr[3] : m2 ? gr[2] : m1 ? gr[1] : gr[0];
    float sq = xsum32(xsum16(dia ? dg : 0.f));
    mean1 = sm[0] * (1.0f / D);
    float var = fmaxf(__builtin_fmaf(sq, 1.0f / D, -mean1 * mean1), 0.f);
    rstd1 = __builtin_amdgcn_rsqf(var + EPS);
    float r12 = rstd1 * __builtin_amdgcn_rsqf(__builtin_fmaf(var * rstd1, rstd1, EPS));
    *(LAS f32x2*)(sta) = f32x2{mean1, rstd1};
    const float k0 = GC0 * r12, k1 = GC1 * r12 * r12 * r12, ri = __builtin_amdgcn_rcpf(r12);
    const f32x4 m4 = {-mean1, -mean1, -mean1, -mean1};
    a0 = __builtin_elementwise_fma(m4, c0, a0); a1 = __builtin_elementwise_fma(m4, c1, a1);
    u32x4 v = {gelu2(f32x2{a0[0], a0[1]}, k0, k1, ri), gelu2(f32x2{a0[2], a0[3]}, k0, k1, ri),
               gelu2(f32x2{a1[0], a1[1]}, k0, k1, ri), gelu2(f32x2{a1[2], a1[3]}, k0, k1, ri)};
    st128(v, rw, WS_H1 + so12);
  }
  STAMP(2);

  u32x2 xr0, xr1;
  u32 xf[8];
  cf bu[8], pre, lpw, lam;
  u32 tlo, thi;
  int hso;
  {
    f32x4 ar0 = {0, 0, 0, 0}, ar1 = ar0, ai0 = ar0, ai1 = ar0;
    u32x4 X0[4], X1[4], FR[4], FI[4];
    u32x2 tg;
    auto mm = [&](int h) {
      ar1 = mfma16(X1[h], FR[h], ar1); ai0 = mfma16(X0[h], FI[h], ai0); ai1 = mfma16(X1[h], FI[h], ai1);
      ar0 = mfma16(X0[h + 1], FR[h + 1], ar0); ar1 = mfma16(X1[h + 1], FR[h + 1], ar1);
      ai0 = mfma16(X0[h + 1], FI[h + 1], ai0); ai1 = mfma16(X1[h + 1], FI[h + 1], ai1);
      PIN4(ar0, ar1, ai0, ai1);
    };
    sfor<0, 8>([&](auto SS) {
      constexpr int s = SS.v, h = 2 * (s & 1), o = h ^ 2;
      constexpr u32 ab = LDS_A + s * SLOT;
      if (s == 2) bard(); else bar();
      if (s) { if (s > 1) PIN1(ar0); ar0 = mfma16(X0[o], FR[o], ar0); PIN1(ar0); }
      X0[h] = lds128(ab + ab0); FR[h] = lds128(WADR(bbo, P_GB + s)); X1[h] = lds128(ab + 512 + ab1); FI[h] = lds128(WADR(bbo, P_GB + s) + SLOT);
      X0[h + 1] = lds128(ab + ab1); FR[h + 1] = lds128(WADR(bbx, P_GB + s)); X1[h + 1] = lds128(ab + 512 + ab0); FI[h + 1] = lds128(WADR(bbx, P_GB + s) + SLOT);
      if (s == 0) { xr0 = lds64(LDS_A + xra); xr1 = lds64(LDS_A + (xra ^ 32)); tg = lds64(SC_TAG); }
      if (s == 0) {
#pragma unroll
        for (int r = 0; r < 8; ++r) xf[r] = lds16(LDS_A + xfa[r >> 1] + (r & 1) * 128);
      }
      if (s > 1) PIN4(ar0, ar1, ai0, ai1); else if (s) PIN1(ar0);
      if (s) mm(o);
    });
    bar();
    f32x4 st[4];
#pragma unroll
    for (int r = 0; r < 4; ++r) st[r] = *(const LAS f32x4*)(stb + r * 16);
    const f32x4 t0 = *(const LAS f32x4*)(lta), t1 = *(const LAS f32x4*)(lta + 16);
    const cf lpg = *(const LAS f32x2*)(lga), csb = *(const LAS f32x2*)(cso);
    PIN4(ar0, ar1, ai0, ai1);
    ar0 = mfma16(X0[2], FR[2], ar0); mm(2);
    STAMP(3);
    tlo = __builtin_amdgcn_readfirstlane(tg[0]); thi = __builtin_amdgcn_readfirstlane(tg[1]);
    lam = cf{t0[0], t0[1]};
    const cf coef = {t0[2], t0[3]}, l8 = {t1[0], t1[1]}, l32 = {t1[2], t1[3]};
    const float csr = csb[0], csi = csb[1];
    cf e8;
#pragma unroll
    for (int rr = 0; rr < 8; ++rr) {
      float m = st[rr >> 1][2 * (rr & 1)], rs = st[rr >> 1][2 * (rr & 1) + 1];
      cf z = {__builtin_fmaf(-m, csr, rr < 4 ? ar0[rr & 3] : ar1[rr & 3]), __builtin_fmaf(-m, csi, rr < 4 ? ai0[rr & 3] : ai1[rr & 3])};
      bu[rr] = cmul(coef, z * rs);
      e8 = rr ? cmad(lam, e8, bu[rr]) : bu[0];
    }
    cf v0, v1, v2, v3;
    {
      auto a = __builtin_amdgcn_permlane16_swap(__float_as_uint(e8[0]), __float_as_uint(e8[0]), false, false);
      auto a0 = __builtin_amdgcn_permlane32_swap(a[0], a[0], false, false), a1 = __builtin_amdgcn_permlane32_swap(a[1], a[1], false, false);
      auto d = __builtin_amdgcn_permlane16_swap(__float_as_uint(e8[1]), __float_as_uint(e8[1]), false, false);
      auto d0 = __builtin_amdgcn_permlane32_swap(d[0], d[0], false, false), d1 = __builtin_amdgcn_permlane32_swap(d[1], d[1], false, false);
      v0 = cf{__uint_as_float(a0[0]), __uint_as_float(d0[0])}; v1 = cf{__uint_as_float(a1[0]), __uint_as_float(d1[0])};
      v2 = cf{__uint_as_float(a0[1]), __uint_as_float(d0[1])}; v3 = cf{__uint_as_float(a1[1]), __uint_as_float(d1[1])};
    }
    LANEVARS(ln);
    cf p2 = cmad(l8, v0, v1), p3 = cmad(l8, p2, v2), p4 = cmad(l8, p3, v3);
    pre = g == 0 ? cf{0.f, 0.f} : g == 1 ? v0 : g == 2 ? p2 : p3;
    lpw = lpg;
    if (tb == 0) *(LAS f32x2*)(SC_E1 + (16 * cg + i16) * 8) = p4;
    hso = WS_H + c * 131072 + j * 16384 + (2 * cg + tb) * 1024 + g * 256 + ((i16 ^ (g << 2)) << 4);
    bar();
    if (tb == 1) {
      cf e32 = *(const LAS f32x2*)(SC_E1 + (16 * cg + i16) * 8);
      pre = cmad(lpg, e32, pre);
      lpw = cmul(lpg, l32);
      cf e64 = cmad(l32, e32, p4);
      if (g == 0) st128(u32x4{__float_as_uint(e64[0]), tlo, __float_as_uint(e64[1]), thi}, rw, WS_E + (c * 512 + chn) * 16);
    }
    PIN1(hso);
    STAMP(4);
  }

  u32x4 WR[16];
  u32 w3o, w3oh;
  {
    f32x4 a0, a1;
    {
      const float mr = -mean1 * rstd1;
      a0 = f32x4{__builtin_fmaf(bflo(xr0[0]), rstd1, mr), __builtin_fmaf(bfhi(xr0[0]), rstd1, mr),
                 __builtin_fmaf(bflo(xr0[1]), rstd1, mr), __builtin_fmaf(bfhi(xr0[1]), rstd1, mr)};
      a1 = f32x4{__builtin_fmaf(bflo(xr1[0]), rstd1, mr), __builtin_fmaf(bfhi(xr1[0]), rstd1, mr),
                 __builtin_fmaf(bflo(xr1[1]), rstd1, mr), __builtin_fmaf(bfhi(xr1[1]), rstd1, mr)};
    }
    u32x4 X[4], F0[4], F1[4];
    u32 wo0h = wo0 + 4 * GRP, wo1h = wo1 + 4 * GRP;
    {
      LANEVARS(ln);
      w3o = LDS_W + (8 * hh + qq) * 448 + 64 * (w < 7 ? w : 0) + 32 * blk + 16 * (pp & 1) + 8 * (pp >> 1); w3oh = w3o + 4 * GRP;
    }
    so12 += WS_X1;
    PIN4(wo0h, wo1h, w3o, w3oh); PIN1(so12);
    auto ld = [&](int q, int s) {
      X[q] = lds128(LDS_A + s * GRP + a2o + q * 4096);
      F0[q] = ldstr2(WADR(wo0, P_G2 + s) + q * 4096, WADR(wo0, P_G2 + s) + q * 4096 + 512);
      F1[q] = ldstr2(WADR(wo1, P_G2 + s) + q * 4096, WADR(wo1, P_G2 + s) + q * 4096 + 512);
    };
    auto mm = [&](int q) { PIN2(a0, a1); a0 = mfma16(F0[q], X[q], a0); a1 = mfma16(F1[q], X[q], a1); PIN2(a0, a1); };
    sfor<0, 4>([&](auto SS) {
      constexpr int s = SS.v;
      bar();
      ld(0, s); ld(1, s);
      if (s) mm(2);
      ld(2, s);
      if (s) mm(3);
      ld(3, s);
      mm(0); mm(1);
    });
    mm(2); mm(3);
    STAMP(5);
    st128(u32x4{pack2(a0[0], a0[1]), pack2(a0[2], a0[3]), pack2(a1[0], a1[1]), pack2(a1[2], a1[3])}, rw, so12);
    auto ldw = [&](auto Q) {
      constexpr int q = Q.v;
      WR[2 * q] = ldstr2(WADR(w3o, P_R + q), WADR(w3o, P_R + q) + 4 * 448);
      WR[2 * q + 1] = ldstr2(WADR(w3o, P_R + q) + SLOT, WADR(w3o, P_R + q) + SLOT + 4 * 448);
    };
    bar();
    sfor<0, 3>(ldw);
    bard();
    STAMP(6);
    bar();
    sfor<3, 8>(ldw);
    const f32x4 hq = *(const LAS f32x4*)(SC_HIN + 2 * (cso - SC_CSB));
    bar();
    cf h = cmad(lpw, cf{hq[0] + hq[2], hq[1] + hq[3]}, pre);
    float hr[8], hi[8];
#pragma unroll
    for (int r = 0; r < 8; ++r) { h = cmad(lam, h, bu[r]); hr[r] = h[0]; hi[r] = -h[1]; }
    st128(u32x4{pack2(hr[0], hr[1]), pack2(hr[2], hr[3]), pack2(hr[4], hr[5]), pack2(hr[6], hr[7])}, rw, hso);
    st128(u32x4{pack2(hi[0], hi[1]), pack2(hi[2], hi[3]), pack2(hi[4], hi[5]), pack2(hi[6], hi[7])}, rw, hso + 8192);
    STAMP(7);
  }
  u32 a3o, x1a, aco, acx, bco, bc1, bc2, bc3, a4o, b4o, xo_, cva, s3a, stc;
  int h3o, oo;
  {
    LANEVARS(ln);
    a3o = w < 7 ? hh * 1024 + l31 * 16 : g * 1024 + i16 * 16;
    x1a = j * SLOT + (4 * nh4 + ((l31 >> 2) & 3)) * 1024 + tf0 * 16 + (4 * (l31 >> 4) + (l31 & 3)) * 2;
    h3o = WS_H3 + c * 229376 + j * 28672 + (4 * w + hh) * 1024 + l31 * 16;
    const int q = 2 * (blk ^ kh4) + (pp >> 1), n = 32 * nh4 + l31;
    aco = kh4 * SLOT + th4 * 1024 + q * 256 + (((8 * hh + qq) ^ (q << 2)) << 4) + (pp & 1) * 8; acx = aco ^ 64;
    bco = LDS_W + kh4 * SLOT + n * 128 + ((hh ^ ((n >> 1) & 7)) << 4); bc1 = bco ^ 32; bc2 = bco ^ 64; bc3 = bco ^ 96;
    a4o = kh4 * SLOT + hh * 1024 + (32 * th4 + (l31 ^ (16 * kh4))) * 16;
    b4o = LDS_W + kh4 * SLOT + (8 * hh + qq) * 128 + 64 * (nh4 ^ (qq >> 1)) + 32 * blk + 8 * pp;
    oo = ((c * 64 + tf0) * D + 64 * j + 32 * nh4 + l31) * 4;
    xo_ = LDS_W + w * 2048 + ln * 16;
    cva = SC_CS3 + w * 128 + 32 * hh; s3a = SC_STAT3 + (w < 7 ? l31 : i16) * 16; stc = SC_STAT + tf0 * 8;
    PIN6(a3o, x1a, h3o, aco, acx, bco); PIN6(bc1, bc2, bc3, a4o, b4o, oo); PIN4(xo_, cva, s3a, stc);
  }
  float* outp = p.out;
  asm volatile("" : "+s"(outp));
  asm volatile("" ::"v"(touch));

  u32 x1f[8];
  auto get_x1f = [&]() {
#pragma unroll
    for (int r = 0; r < 8; ++r) x1f[r] = lds16(LDS_A + x1a + (8 * (r >> 2) + (r & 3)) * 16);
  };
  f32x16 g0, g1;
#pragma unroll
  for (int r = 0; r < 16; ++r) { g0[r] = 0.f; g1[r] = 0.f; }
  if (w < 7) {
    u32x4 W[2], X0[4], X1[4];
    sfor<0, 8>([&](auto SS) {
      constexpr int s = SS.v;
      constexpr u32 ab = LDS_A + s * SLOT;
      if (s == 1) bard(); else bar();
      if (s) { PIN1(g0); g0 = mfma32(W[1], X0[3], g0); PIN1(g0); }
      X0[0] = lds128(ab + a3o); X1[0] = lds128(ab + a3o + 512); X0[1] = lds128(ab + a3o + 2048); X1[1] = lds128(ab + a3o + 2048 + 512);
      W[0] = ldstr2(WADR(w3o, P_G3 + s), WADR(w3o, P_G3 + s) + 4 * 448); X0[2] = lds128(ab + a3o + 4096); X1[2] = lds128(ab + a3o + 4096 + 512);
      X0[3] = lds128(ab + a3o + 6144);
      if (s) { PIN1(g1); g1 = mfma32(W[1], X1[3], g1); PIN1(g1); }
      W[1] = ldstr2(WADR(w3o, P_G3 + s) + SLOT, WADR(w3o, P_G3 + s) + SLOT + 4 * 448); X1[3] = lds128(ab + a3o + 6144 + 512);
      if (s == 4) get_x1f();
      if (s) PIN2(g0, g1); else asm volatile("" ::: "memory");
      g0 = mfma32(WR[2 * s], X0[0], g0); g1 = mfma32(WR[2 * s], X1[0], g1);
      g0 = mfma32(WR[2 * s + 1], X0[1], g0); g1 = mfma32(WR[2 * s + 1], X1[1], g1);
      g0 = mfma32(W[0], X0[2], g0); g1 = mfma32(W[0], X1[2], g1);
      PIN2(g0, g1);
    });
    g0 = mfma32(W[1], X0[3], g0); g1 = mfma32(W[1], X1[3], g1);
  } else {
    f32x4 sm[4], gr[4];
#pragma unroll
    for (int q = 0; q < 4; ++q) sm[q] = gr[q] = f32x4{0, 0, 0, 0};
    auto slab = [&](int s) {
      u32x4 v[8];
#pragma unroll
      for (int q = 0; q < 8; ++q) v[q] = lds128(LDS_A + s * SLOT + a3o + (q >> 2) * 4096 + (q & 3) * 256);
#pragma unroll
      for (int q = 0; q < 8; ++q) { sm[q & 3] = mfma16(ones, v[q], sm[q & 3]); gr[q & 3] = mfma16(v[q], v[q], gr[q & 3]); }
    };
    bar();
    slab(0);
    drain();
#pragma unroll 1
    for (int s = 1; s < 8; ++s) {
      slab(s);
      if (s == 5) get_x1f();
      bar();
    }
#pragma unroll
    for (int q = 0; q < 4; ++q) {
      float dg = m3 ? gr[q][3] : m2 ? gr[q][2] : m1 ? gr[q][1] : gr[q][0];
      float m = sm[q][0] * (1.0f / D), ve = fmaxf(__builtin_fmaf(dg, 1.0f / D, -m * m), 0.f) + EPS, r = __builtin_amdgcn_rsqf(ve);
      if (dia) *(LAS f32x4*)(s3a + q * 256) = f32x4{-m, GC0 * r, GC1 * r * r * r, ve * r};
    }
  }
  STAMP(8);

  float sv[8];
  {
    f32x16 a;
#pragma unroll
    for (int r = 0; r < 16; ++r) a[r] = 0.f;
    u32x4 X[4], W[4];
    f32x4 cv[4], s3[2];
    u32 bcoh = bco + 4 * GRP, bc1h = bc1 + 4 * GRP, bc2h = bc2 + 4 * GRP, bc3h = bc3 + 4 * GRP;
    PIN4(bcoh, bc1h, bc2h, bc3h);
    u32x4 v;
    auto gel = [&](int th, int sx, int r) {
      f32x2 z = th ? f32x2{g1[8 * sx + 2 * r], g1[8 * sx + 2 * r + 1]} : f32x2{g0[8 * sx + 2 * r], g0[8 * sx + 2 * r + 1]};
      f32x2 cc = {cv[2 * sx + (r >> 1)][2 * (r & 1)], cv[2 * sx + (r >> 1)][2 * (r & 1) + 1]};
      v[r] = gelu2(__builtin_elementwise_fma(f32x2{s3[th][0], s3[th][0]}, cc, z), s3[th][1], s3[th][2], s3[th][3]);
    };
    sfor<0, 8>([&](auto SS) {
      constexpr int s = SS.v;
      constexpr u32 ab = LDS_A + (s & 3) * GRP;
      if (s == 5) bard(); else bar();
      if (s == 0) {
#pragma unroll
        for (int q = 0; q < 4; ++q) cv[q] = *(const LAS f32x4*)(cva + 64 * (q >> 1) + 16 * (q & 1));
        s3[0] = *(const LAS f32x4*)(s3a); s3[1] = *(const LAS f32x4*)(s3a + 512);
      }
      if (s) { PIN1(a); a = mfma32(X[2], W[2], a); PIN1(a); }
      X[0] = ldstr2(ab + aco, ab + acx); W[0] = lds128(WADR(bco, P_GC + s));
      X[1] = ldstr2(ab + aco + 2048, ab + acx + 2048); W[1] = lds128(WADR(bc1, P_GC + s));
      if (s) { PIN1(a); a = mfma32(X[3], W[3], a); PIN1(a); }
      X[2] = ldstr2(ab + aco + 4096, ab + acx + 4096); W[2] = lds128(WADR(bc2, P_GC + s));
      X[3] = ldstr2(ab + aco + 6144, ab + acx + 6144); W[3] = lds128(WADR(bc3, P_GC + s));
      if (s < 4 && w < 7) { gel(s >> 1, s & 1, 0); gel(s >> 1, s & 1, 1); PIN1(v); }
      if (s) PIN1(a); else asm volatile("" ::: "memory");
      a = mfma32(X[0], W[0], a); PIN1(a);
      if (s < 4 && w < 7) { gel(s >> 1, s & 1, 2); gel(s >> 1, s & 1, 3); st128(v, rw, h3o + (s & 1) * 2048 + (s >> 1) * 512); }
      if (s == 0 && w == 7 && b == 0) {
        const char* fp = p.ws + WS_FLAG + FL_H1 * 32768 + lane * 128;
        const u64 tag = ((u64)thi << 32) | tlo;
#pragma unroll 1
        for (u32 sp = 0; sp < SPIN_CAP; ++sp) {
          u64 v0 = ld_u64(fp), v1 = ld_u64(fp + 8192), v2 = ld_u64(fp + 16384), v3 = ld_u64(fp + 24576);
          if (__all(v0 == tag && v1 == tag && v2 == tag && v3 == tag)) break;
          __builtin_amdgcn_s_sleep(1);
        }
        if (lane == 0) st_u64(p.ws + WS_EPOCH, tag);
      }
      PIN1(a); a = mfma32(X[1], W[1], a); PIN1(a);
      if (s == 3) STAMP(9);
    });
    f32x4 st[4];
#pragma unroll
    for (int r = 0; r < 4; ++r) st[r] = *(const LAS f32x4*)(stc + (8 * (r >> 1) + 2 * (r & 1)) * 8);
    a = mfma32(X[2], W[2], a); a = mfma32(X[3], W[3], a);
    STAMP(10);
    const float dskip = bf2f(q_dk);
#pragma unroll
    for (int r = 0; r < 8; ++r) sv[r] = (bf2f(xf[r]) - st[r >> 1][2 * (r & 1)]) * st[r >> 1][2 * (r & 1) + 1] * dskip;
    *(LAS f32x4*)(xo_ + wgr(P_XC)) = f32x4{a[8], a[9], a[10], a[11]};
    *(LAS f32x4*)(xo_ + wgr(P_XC) + 1024) = f32x4{a[12], a[13], a[14], a[15]};
    bar();
    f32x4 o0 = *(const LAS f32x4*)((xo_ ^ 8192) + wgr(P_XC)), o1 = *(const LAS f32x4*)((xo_ ^ 8192) + wgr(P_XC) + 1024);
#pragma unroll
    for (int r = 0; r < 8; ++r) sv[r] = __builtin_fmaf(2.0f, a[r] + (r < 4 ? o0[r & 3] : o1[r & 3]), sv[r]);
    PIN4(sv[0], sv[1], sv[2], sv[3]); PIN4(sv[4], sv[5], sv[6], sv[7]);
    STAMP(11);
  }

  {
    f32x16 a;
#pragma unroll
    for (int r = 0; r < 16; ++r) a[r] = r < 8 ? bf2f(x1f[r]) : 0.f;
    float xo[8];
#pragma unroll
    for (int r = 0; r < 8; ++r) xo[r] = bf2f(xf[r]);
    u32 b4oh = b4o + 4 * GRP, xg = xo_ ^ 8192;
    PIN4(xo[0], xo[1], xo[2], xo[3]); PIN6(xo[4], xo[5], xo[6], xo[7], b4oh, xg);
    u32x4 X[4], W[4];
    sfor<0, 14>([&](auto SS) {
      constexpr int s = SS.v;
      constexpr u32 ab = LDS_A + (s & 3) * GRP;
      bar();
      if (s) { PIN1(a); a = mfma32(X[2], W[2], a); PIN1(a); }
#pragma unroll
      for (int ks = 0; ks < 2; ++ks) { X[ks] = lds128(ab + a4o + ks * 2048); W[ks] = ldstr2(WADR(b4o, P_G4 + s) + ks * 2048, WADR(b4o, P_G4 + s) + ks * 2048 + 512); }
      if (s) { PIN1(a); a = mfma32(X[3], W[3], a); PIN1(a); }
#pragma unroll
      for (int ks = 2; ks < 4; ++ks) { X[ks] = lds128(ab + a4o + ks * 2048); W[ks] = ldstr2(WADR(b4o, P_G4 + s) + ks * 2048, WADR(b4o, P_G4 + s) + ks * 2048 + 512); }
      PIN1(a); a = mfma32(X[0], W[0], a); a = mfma32(X[1], W[1], a); PIN1(a);
    });
    a = mfma32(X[2], W[2], a); a = mfma32(X[3], W[3], a);
    STAMP(12);
    *(LAS f32x4*)(xo_ + wgr(P_X4)) = f32x4{a[8], a[9], a[10], a[11]};
    *(LAS f32x4*)(xo_ + wgr(P_X4) + 1024) = f32x4{a[12], a[13], a[14], a[15]};
    bar();
    f32x4 o0 = *(const LAS f32x4*)(xg + wgr(P_X4)), o1 = *(const LAS f32x4*)(xg + wgr(P_X4) + 1024);
    const rsrc_t ro = mkrsrc(outp, S * D * 4);
#pragma unroll
    for (int r = 0; r < 8; ++r) {
      float z = a[r] + (r < 4 ? o0[r & 3] : o1[r & 3]);
      float gsl = z * __builtin_amdgcn_rcpf(1.0f + __builtin_amdgcn_exp2f(-1.4426950408889634f * z));
      float res = __builtin_fmaf(sv[r], gsl, xo[r]);
      __builtin_amdgcn_raw_buffer_store_b32(__float_as_uint(res), ro, oo + (r & 1) * 2048, (8 * (r >> 2) + (r & 2)) * D * 4, 0);
    }
    STAMP(13);
    STAMP_OUT();
  }
}
}

extern "C" void kernel_launch(void* const* d_in, const int* in_sizes, int n_in, void* d_out, int out_size, void* d_ws,
                              size_t ws_size, hipStream_t stream) {
  using namespace s5k;
  Params p;
  p.x = (const u16*)d_in[0]; p.lre = (const u16*)d_in[1]; p.lim = (const u16*)d_in[2];
  p.bre = (const u16*)d_in[3]; p.bim = (const u16*)d_in[4]; p.cre = (const u16*)d_in[5]; p.cim = (const u16*)d_in[6];
  p.ldt = (const u16*)d_in[7]; p.dsk = (const u16*)d_in[8];
  p.w1 = (const u16*)d_in[10]; p.w2 = (const u16*)d_in[11]; p.w3 = (const u16*)d_in[12]; p.w4 = (const u16*)d_in[13];
  p.out = (float*)d_out; p.ws = (char*)d_ws;
  (void)hipFuncSetAttribute((const void*)fused_kernel, hipFuncAttributeMaxDynamicSharedMemorySize, LDS_BYTES);
  fused_kernel<<<256, NTHR, LDS_BYTES, stream>>>(p);
}
```

```cpp
#include <hip/hip_runtime.h>
#include <hip/hip_bf16.h>
#include <stdint.h>

#if defined(__HIP_DEVICE_COMPILE__)
#pragma clang attribute push(__attribute__((target("no-packed-fp32-ops"))), apply_to = function)
#endif
namespace s5k {
typedef unsigned short u16;
typedef unsigned int u32;
typedef unsigned long long u64;
typedef __attribute__((ext_vector_type(4))) short s16x4;
typedef __attribute__((ext_vector_type(2))) __bf16 bf16x2;
typedef __attribute__((ext_vector_type(8))) __bf16 bf16x8;
typedef __attribute__((ext_vector_type(2))) float f32x2;
typedef __attribute__((ext_vector_type(4))) float f32x4;
typedef __attribute__((ext_vector_type(16))) float f32x16;
typedef __attribute__((ext_vector_type(2))) u32 u32x2;
typedef __attribute__((ext_vector_type(4))) u32 u32x4;
#define LAS __attribute__((address_space(3)))
#define GAS __attribute__((address_space(1)))
#define DI __device__ __forceinline__
typedef __amdgpu_buffer_rsrc_t rsrc_t;

constexpr int S = 2048, D = 512, F = 1792;
constexpr float EPS = 1e-5f;
constexpr int NTHR = 768;
constexpr int SLOT = 8192, GRP = 16384, NWG = 5;
constexpr int LDS_A = 0, LDS_W = 4 * GRP, LDS_S = LDS_W + NWG * GRP, LDS_BYTES = 163840;
constexpr int SC_TAG = LDS_S;
constexpr int SC_STAT = LDS_S + 256;
constexpr int SC_CS1 = LDS_S + 768;
constexpr int SC_CS3 = LDS_S + 1280;
constexpr int SC_GEL = LDS_S + 2304;
constexpr int SC_CSB = LDS_S + 3584;
constexpr int SC_LG = LDS_S + 4096;
constexpr int SC_JUNK = LDS_S + 3328;
constexpr int SC_X1 = LDS_S + 8192;
constexpr int WS_EPOCH = 0;
constexpr int WS_FLAG = 4096;
constexpr int FL_H1 = 0, FL_X1 = 1, FL_H = 2, FL_H3 = 3, FL_H3Q = 4;
constexpr int WS_E = WS_FLAG + 5 * 256 * 128;
constexpr int WS_CS3 = WS_E + 32 * 512 * 16;
constexpr int WS_H1 = WS_CS3 + 56 * 8 * 256;
constexpr int WS_X1 = WS_H1 + 32 * 65536;
constexpr int WS_H = WS_X1 + 32 * 65536;
constexpr int WS_H3 = WS_H + 32 * 131072;
constexpr int WS_END = WS_H3 + 32 * 229376;
constexpr int OOB = 0x40000000;
constexpr int SC1 = 16;
constexpr u32 SPIN_CAP = 1u << 16;

struct Params {
  const u16 *x, *lre, *lim, *bre, *bim, *cre, *cim, *ldt, *dsk, *w1, *w2, *w3, *w4;
  float* out;
  char* ws;
};

DI float u2f(u32 v) { return __builtin_bit_cast(float, v); }
DI u32 f2u(float v) { return __builtin_bit_cast(u32, v); }
DI bool wall(bool c) { return __builtin_amdgcn_ballot_w64(!c) == 0; }
DI rsrc_t mkrsrc(const void* p, int bytes) { return __builtin_amdgcn_make_buffer_rsrc((void*)p, 0, bytes, 0x00020000); }
DI float bf2f(u32 v) { return u2f(v << 16); }
DI float bflo(u32 v) { return u2f(v << 16); }
DI float bfhi(u32 v) { return u2f(v & 0xffff0000u); }
DI u32 pack2(float a, float b) { f32x2 v = {a, b}; return __builtin_bit_cast(u32, __builtin_convertvector(v, bf16x2)); }
constexpr u32 ONES = 0x3f803f80u;
DI float xsum32(float x) { auto r = __builtin_amdgcn_permlane32_swap(f2u(x), f2u(x), false, false); return u2f(r[0]) + u2f(r[1]); }
DI float xsum16(float x) { auto r = __builtin_amdgcn_permlane16_swap(f2u(x), f2u(x), false, false); return u2f(r[0]) + u2f(r[1]); }
DI f32x4 mfma16(u32x4 a, u32x4 b, f32x4 c) {
  return __builtin_amdgcn_mfma_f32_16x16x32_bf16(__builtin_bit_cast(bf16x8, a), __builtin_bit_cast(bf16x8, b), c, 0, 0, 0);
}
DI f32x16 mfma32(u32x4 a, u32x4 b, f32x16 c) {
  return __builtin_amdgcn_mfma_f32_32x32x16_bf16(__builtin_bit_cast(bf16x8, a), __builtin_bit_cast(bf16x8, b), c, 0, 0, 0);
}
DI u32x4 lds128(u32 a) { return *(const LAS u32x4*)(a); }
DI u32x2 ldstr(u32 a) { return __builtin_bit_cast(u32x2, __builtin_amdgcn_ds_read_tr16_b64_v4i16((LAS s16x4*)(a))); }
DI u32x2 lds64(u32 a) { return *(const LAS u32x2*)(a); }
DI u32 lds16(u32 a) { return *(const LAS u16*)(a); }
DI u32x4 ldstr2(u32 a0, u32 a1) { u32x2 x = ldstr(a0), y = ldstr(a1); return u32x4{x[0], x[1], y[0], y[1]}; }
#define bar() do { asm volatile("" ::: "memory"); __builtin_amdgcn_s_waitcnt(0xC07F); asm volatile("s_barrier" ::: "memory"); } while (0)
#define barn(n) do { asm volatile("" ::: "memory"); __builtin_amdgcn_s_waitcnt(0xC07F | ((n) << 8)); asm volatile("s_barrier" ::: "memory"); } while (0)
#define bard() do { asm volatile("" ::: "memory"); __builtin_amdgcn_s_waitcnt(0x0070); asm volatile("s_waitcnt vmcnt(0)" ::: "memory"); asm volatile("s_barrier" ::: "memory"); } while (0)
#define lbar() do { asm volatile("s_barrier" ::: "memory"); } while (0)
DI void st128(u32x4 v, rsrc_t rs, int off) { __builtin_amdgcn_raw_buffer_store_b128(v, rs, off, 0, SC1); }
DI void drain() { asm volatile("s_waitcnt vmcnt(0)" ::: "memory"); }
constexpr float GC0 = -2.0f * 0.7978845608028654f * 1.4426950408889634f, GC1 = GC0 * 0.044715f;
DI int swx(int t) { return ((t >> 1) & 1) | ((t >> 3) & 2) | ((t ^ (t >> 3)) & 4); }
typedef f32x2 cf;
DI cf cmad(cf a, cf b, cf c) {
  cf t = __builtin_elementwise_fma(cf{a[0], a[0]}, b, c);
  return __builtin_elementwise_fma(cf{-a[1], a[1]}, cf{b[1], b[0]}, t);
}
DI cf cmul(cf a, cf b) { return __builtin_elementwise_fma(cf{-a[1], a[1]}, cf{b[1], b[0]}, cf{a[0], a[0]} * b); }
DI cf cpow(float la, float lb, float n) {
  float m = __builtin_amdgcn_exp2f(n * la), f = __builtin_amdgcn_fractf(n * lb);
  return cf{m * __builtin_amdgcn_cosf(f), m * __builtin_amdgcn_sinf(f)};
}
#define PIN1(a) asm volatile("" : "+v"(a)::"memory")
#define PIN2(a, b) asm volatile("" : "+v"(a), "+v"(b)::"memory")
#define PIN3(a, b, c) asm volatile("" : "+v"(a), "+v"(b), "+v"(c)::"memory")
#define PIN4(a, b, c, d) asm volatile("" : "+v"(a), "+v"(b), "+v"(c), "+v"(d)::"memory")
#define PIN6(a, b, c, d, e, f) asm volatile("" : "+v"(a), "+v"(b), "+v"(c), "+v"(d), "+v"(e), "+v"(f)::"memory")
DI void st_u64(char* p, u64 v) { __hip_atomic_store((GAS u64*)p, v, __ATOMIC_RELAXED, __HIP_MEMORY_SCOPE_AGENT); }
DI u64 make_tag(u64 e) {
  u64 z = e + 0x9E3779B97F4A7C15ull;
  z = (z ^ (z >> 30)) * 0xBF58476D1CE4E5B9ull;
  z = (z ^ (z >> 27)) * 0x94D049BB133111EBull;
  z ^= z >> 31;
  u32 lo = (u32)z, hi = (u32)(z >> 32);
  lo |= 1u; hi |= 1u;
  if (lo == (u32)e) lo ^= 0x5bd1e994u;
  if (hi == (u32)(e >> 32)) hi ^= 0x5bd1e994u;
  return ((u64)hi << 32) | lo;
}

template <int V> struct IC { static constexpr int v = V; };
template <int I, int N, class Fn> DI void sfor(Fn&& f) {
  if constexpr (I < N) { f(IC<I>{}); sfor<I + 1, N>(f); }
}
template <int N> DI void wait_vm() { asm volatile("s_waitcnt vmcnt(%0)" ::"n"(N) : "memory"); }
template <int AUX> DI void dma4(rsrc_t rs, u32 lds, int v0, int v1, int v2, int v3, int so) {
  __builtin_amdgcn_raw_ptr_buffer_load_lds(rs, (LAS void*)lds, 16, v0, so, 0, AUX);
  __builtin_amdgcn_raw_ptr_buffer_load_lds(rs, (LAS void*)lds, 16, v1, so, 1024, AUX);
  __builtin_amdgcn_raw_ptr_buffer_load_lds(rs, (LAS void*)lds, 16, v2, so, 2048, AUX);
  __builtin_amdgcn_raw_ptr_buffer_load_lds(rs, (LAS void*)lds, 16, v3, so, 3072, AUX);
}
DI void lds_put64(u32 a, u32x2 v) { asm volatile("ds_write_b64 %0, %1" ::"v"(a), "v"(v) : "memory"); }
DI u32 xor_or(u32 a, u32 b, u32 c) { return __builtin_amdgcn_bitop3_b32(a, b, c, 0xBE); }
DI u64 peek(const char* p) { u64 v; asm volatile("global_load_dwordx2 %0, %1, off sc1" : "=v"(v) : "v"(p) : "memory"); return v; }
template <int N> DI void settle(u64& v) { asm volatile("s_waitcnt vmcnt(%1)" : "+v"(v) : "n"(N) : "memory"); }
DI void spin(const char* p, bool use, u64 v, u64 tag) {
#pragma unroll 1
  for (u32 sp = 0; sp < SPIN_CAP && !wall(!use || v == tag); ++sp) { v = peek(p); settle<0>(v); }
}
template <int N> DI void l2_pull(const void* base, int bytes, int grp, int lane) {
  const rsrc_t rs = mkrsrc(base, bytes);
  const int vo = (grp * N * 64 + lane) * 128;
#pragma unroll
  for (int i = 0; i < N; ++i)
    asm volatile("s_mov_b32 m0, %0\n\ts_nop 0\n\tbuffer_load_dword %1, %2, %3 offen lds" ::"s"(SC_JUNK), "v"(vo), "s"(rs), "s"(i * 8192) : "memory", "m0");
}
DI void slow_pull(const void* base, int bytes, int grp, int n, int lane) {
  const int js = (3 - (int)((unsigned long long)base >> 7)) & 7;
  const int vo = lane < n ? (grp * n + lane) * 1024 : OOB;
  asm volatile("s_mov_b32 m0, %0\n\ts_nop 0\n\tbuffer_load_dword %1, %2, %3 offen lds" ::"s"(SC_JUNK), "v"(vo), "s"(mkrsrc(base, bytes)), "s"(js * 128) : "memory", "m0");
}
typedef const u16* const __attribute__((address_space(4)))* kptr_t;
constexpr int P_GB = 4, P_G2 = 12, P_QR = 16, P_PW = 20, P_PR = 21, P_QW = 28, P_GC = 32, P_G4 = 40, P_END = 54;
constexpr int T_GB = 4, T_P1 = 6, T_E0 = 12, T_G2 = 13, T_RA = 17, T_X1 = 18, T_A = 19, T_B = 26, T_GC = 34, T_P3 = 35, T_P3Q = 40, T_G4 = 42, T_END = 56;
constexpr int pw_grp(int s) { return s == 1 ? 20 : s < 4 ? 20 + s : s == 4 ? 26 : s < 7 ? 19 + s : 27; }
constexpr int pw_pos(int n) { return n == 20 ? 1 : n == 21 ? 0 : n < 24 ? n - 20 : n < 26 ? n - 19 : n == 26 ? 4 : 7; }
constexpr int cmin(int a, int b) { return a < b ? a : b; }
constexpr int NB0 = 2;
constexpr int w_use(int n) {
  return n < P_G2 ? n : n < P_QR ? T_G2 + n - P_G2 : n == P_QR ? T_G2 + 1 : n < P_QR + 3 ? T_RA : n < P_PW ? T_X1 : n == P_PR ? T_RA
       : n < P_QW ? T_A - 1 + pw_pos(n) : n < P_GC ? T_B + n - P_QW : T_GC + n - P_GC;
}
constexpr int w_iss(int n) {
  return n < NB0 ? -1 : n < 4 ? 0 : n < NWG ? 1 : w_use(n - NWG) + 1;
}
constexpr int w_wait(int t) {
  int last = -1;
  for (int n = 0; n < P_END; ++n) if (w_use(n) == t && (last < 0 || w_iss(n) >= w_iss(last))) last = n;
  if (last < 0) return -1;
  int k = 0;
  for (int n = 0; n < P_END; ++n) if (w_iss(n) < t && (w_iss(n) > w_iss(last) || (w_iss(n) == w_iss(last) && n > last))) k += 8;
  return k;
}
constexpr int g4_unit(int q, int part) {
  return q < 8 ? 7 * q + 3 * (q & 1) + part : 7 * ((4 * q - 32 + part) / 3) + 4 - 4 * (((4 * q - 32 + part) / 3) & 1) + (4 * q - 32 + part) % 3;
}
constexpr u32 wgr(int s) { return (s % NWG) * GRP; }

DI void weight_loader(const Params& p, int wl, int lane, int j, const int* v4) {
  const u32 lo = LDS_W + wl * 4096;
  int vb[4], vp[4], vq[4], vw[4];
  const int cp = 448 * j + (j & 1) * 192, cq = 448 * j + 256 - (j & 1) * 256;
  auto issue = [&](auto N) {
    constexpr int n = N.v;
    const u32 dst = lo + wgr(n);
    auto two = [&](rsrc_t ra, rsrc_t rb, const int* v, int so, int d1) {
      dma4<0>(ra, dst, v[0], v[1], v[2], v[3], so);
      dma4<0>(rb, dst + SLOT, v[0], v[1], v[2], v[3], so + d1);
    };
    auto w3 = [&](const int* v, int col, int pos) {
      two(mkrsrc(p.w3, D * F * 2), mkrsrc(p.w3, D * F * 2), v, col + ((j + pos) & 7) * (64 * F * 2), 32 * F * 2);
    };
    if constexpr (n < P_GB) two(mkrsrc(p.w1, D * D * 2), mkrsrc(p.w1, D * D * 2), v4, 128 * j + n * 131072, 65536);
    else if constexpr (n < P_G2) two(mkrsrc(p.bre, D * D * 2), mkrsrc(p.bim, D * D * 2), vb, 65536 * j + (n - P_GB) * 128, 0);
    else if constexpr (n < P_QR) two(mkrsrc(p.w2, D * D * 2), mkrsrc(p.w2, D * D * 2), v4, 128 * j + (n - P_G2) * 131072, 65536);
    else if constexpr (n < P_PW) w3(vq, cq, 4 + n - P_QR);
    else if constexpr (n < P_QW) w3(vp, cp, pw_pos(n));
    else if constexpr (n < P_GC) w3(vq, cq, n - P_QW);
    else if constexpr (n < P_G4) two(mkrsrc(p.cre, D * D * 2), mkrsrc(p.cim, D * D * 2), vb, 65536 * j + (n - P_GC) * 128, 0);
    else {
      dma4<0>(mkrsrc(p.w4, D * F * 2), dst, vw[0], vw[1], vw[2], vw[3], 128 * j + (wl ? g4_unit(n - P_G4, 1) : g4_unit(n - P_G4, 0)) * 32768);
      dma4<0>(mkrsrc(p.w4, D * F * 2), dst + SLOT, vw[0], vw[1], vw[2], vw[3], 128 * j + (wl ? g4_unit(n - P_G4, 3) : g4_unit(n - P_G4, 2)) * 32768);
    }
  };
  sfor<0, T_END>([&](auto T) {
    constexpr int t = T.v;
    if constexpr (w_wait(t) >= 0) wait_vm<w_wait(t)>();
    lbar();
    sfor<0, P_END>([&](auto N) { if constexpr (w_iss(N.v) == t) issue(N); });
    if constexpr (t == 0) {
      int ln = lane; asm volatile("" : "+v"(ln)::"memory");
#pragma unroll
      for (int i = 0; i < 4; ++i) vb[i] = (32 * wl + 7 * i + (ln >> 3)) * 1024 + (((ln & 7) ^ (((ln >> 4) + 4 * i) & 7)) << 4);
      PIN4(vb[0], vb[1], vb[2], vb[3]);
    }
    if constexpr (t == T_GB) {
      int ln = lane; asm volatile("" : "+v"(ln)::"memory");
#pragma unroll
      for (int i = 0; i < 4; ++i) {
        vp[i] = (16 * wl + 4 * i + (ln >> 4)) * (F * 2) + (((ln & 15) ^ ((ln >> 4) << 2)) << 4) - i * 1024;
        vw[i] = v4[i] - 32768 * wl;
        const int ch = (4 * wl + i) * 64 + ln, rr = ch / 12;
        vq[i] = ch < 384 ? rr * (F * 2) + (ch - rr * 12) * 16 - i * 1024 : OOB;
      }
      PIN4(vp[0], vp[1], vp[2], vp[3]); PIN4(vq[0], vq[1], vq[2], vq[3]); PIN4(vw[0], vw[1], vw[2], vw[3]);
    }
  });
}

constexpr int T_PH = T_A + 1, T_H = T_B + 2, T_H3 = T_GC + 5, T_H3Q = T_GC + 13;
constexpr int a_issued(int t) {
  return t < T_H ? 0 : t < T_GC ? (t - T_B) / 2 : cmin(22, 4 + t - T_GC);
}
DI void act_loader(const Params& p, int al, int lane, int b, int c, int j, const int* vo) {
  const u32 lo = LDS_A + al * 4096;
  const rsrc_t rx = mkrsrc(p.x, S * D * 2);
  auto ldx = [&](int m) { dma4<0>(rx, lo + m * SLOT, vo[0], vo[1], vo[2], vo[3], c * 65536 + m * 128); };
  char* const ws = p.ws;
  const rsrc_t rw = mkrsrc(ws, WS_END);
  const int rg = b / 28, cb = b - 28 * rg, i16 = lane & 15;
  u32x4 jb[4];
  u32x2 epv;
  u32 q_lr, q_li, q_dt;
  int vl, vh[4];
  bool use1;
  const bool use0 = lane < 8;
  const char *f1, *f0;
  char* const fme = ws + WS_FLAG + (c * 8 + j) * 128;
  const bool pub = al == 0 && lane == 0;
  auto ldh1 = [&](int m) {
    dma4<SC1>(rw, lo + m * GRP, vh[m], vh[m], vh[m], vh[m], WS_H1 + m * GRP);
    dma4<SC1>(rw, lo + m * GRP + SLOT, vh[m], vh[m], vh[m], vh[m], WS_H1 + m * GRP + SLOT);
  };
  auto ldx1 = [&](int m) { dma4<SC1>(rw, lo + m * SLOT, vl, vl, vl, vl, WS_X1 + c * 65536 + ((j + m) & 7) * SLOT); };
  u64 tag = 0, pk = 0, pk2 = 0;
  bool early = false;
  u64 miss = 0;
  const char* fx = ws + WS_FLAG + FL_X1 * 32768 + (c * 8 + ((j + lane) & 7)) * 128;
  u32 tlo = 0, thi = 0;
  float la, lb;
  cf lam;
  sfor<0, T_END>([&](auto T) {
    constexpr int t = T.v;
    if constexpr (t == 0) wait_vm<8 * (NB0 - 1)>();
    if constexpr (t == 1) wait_vm<16>();
    if constexpr (t == 2 || t == 3) wait_vm<8 * (3 - t) + 8>();
    if constexpr (t >= T_G2 && t < T_G2 + 4) wait_vm<8 * (3 - (t - T_G2))>();
    if constexpr (t == T_A) wait_vm<4>();
    if constexpr (t == T_A + 1) wait_vm<8>();
    if constexpr (t >= T_A + 2 && t < T_A + 6) wait_vm<4 * (5 - (t - T_A))>();
    if constexpr (t >= T_GC) wait_vm<8 * (a_issued(t - 1) - (t - T_GC) - 1)>();
    lbar();
    if constexpr (t == 0) {
#pragma unroll
      for (int m = 2 * NB0; m < 8; ++m) ldx(m);
    }
    if constexpr (t == 1) {
      epv = __builtin_amdgcn_raw_buffer_load_b64(rw, WS_EPOCH, 0, SC1);
      q_lr = ((const GAS u16*)p.lre)[64 * j + lane]; q_li = ((const GAS u16*)p.lim)[64 * j + lane]; q_dt = ((const GAS u16*)p.ldt)[64 * j + lane];
      const rsrc_t r3 = mkrsrc(p.w3, b < 224 ? D * F * 2 : 0);
#pragma unroll
      for (int i = 0; i < 4; ++i)
        jb[i] = __builtin_amdgcn_raw_buffer_load_b128(r3, (lane >> 2) * (F * 2) + (lane & 3) * 16, (64 * rg + 16 * i) * (F * 2) + cb * 128 + al * 64, 0);
    }
    if constexpr (t == 2) {
      int ln = lane; asm volatile("" : "+v"(ln)::"memory");
      vl = al * 4096 + ln * 16;
#pragma unroll
      for (int m = 0; m < 4; ++m) {
        const int tt = 64 * c + ln + ((m & 1) ? (m >> 1) + 1 : -((m >> 1) + 1));
        vh[m] = (tt >= 0 && tt < S) ? (tt >> 6) * 65536 + (tt & 63) * 16 + al * 4096 : OOB;
      }
      const int cn = ln < 8 ? c : c + (((ln >> 1) & 1) ? 1 : -1);
      use1 = ln < 16 && cn >= 0 && cn < 32;
      f1 = ws + WS_FLAG + ((use1 ? cn : c) * 8 + (ln & 7)) * 128;
      f0 = ws + WS_FLAG + (c * 8 + (ln & 7)) * 128;
      PIN4(vh[0], vh[1], vh[2], vh[3]); PIN3(vl, f1, f0);
    }
    if constexpr (t == T_GB - 1) {
      asm volatile("" : "+v"(epv));
      tag = make_tag(((u64)(u32)__builtin_amdgcn_readfirstlane(epv[1]) << 32) | (u32)__builtin_amdgcn_readfirstlane(epv[0]));
      tlo = (u32)tag; thi = (u32)(tag >> 32);
      lds_put64(SC_TAG, u32x2{tlo, thi});
      asm volatile("" : "+v"(q_lr), "+v"(q_li), "+v"(q_dt));
      const float dt = bflo(pack2(__builtin_amdgcn_exp2f(bf2f(q_dt) * 1.4426950408889634f), 0.f));
      la = dt * bf2f(q_lr) * 1.4426950408889634f; lb = dt * bf2f(q_li) * 0.15915494309189535f;
      PIN2(la, lb);
    }
    if constexpr (t == T_GB - 1) {
      __builtin_amdgcn_s_setprio(0);
      u32 one = i16 < 8 ? 0x3f80u << (16 * (i16 & 1)) : 0u;
      u32x4 sel = {(i16 >> 1) == 0 ? one : 0u, (i16 >> 1) == 1 ? one : 0u, (i16 >> 1) == 2 ? one : 0u, (i16 >> 1) == 3 ? one : 0u};
      f32x4 z = {0, 0, 0, 0};
      PIN2(sel, z);
#pragma unroll
      for (int i = 0; i < 4; ++i) z = mfma16(jb[i], sel, z);
      PIN1(z);
      const int o = b < 224 && lane < 8 ? WS_CS3 + ((2 * cb + al) * 8 + rg) * 256 + i16 * 16 : OOB;
      st128(u32x4{f2u(xsum32(xsum16(z[0]))), tlo, f2u(xsum32(xsum16(z[1]))), thi}, rw, o);
      st128(u32x4{f2u(xsum32(xsum16(z[2]))), tlo, f2u(xsum32(xsum16(z[3]))), thi}, rw, o + 128);
      __builtin_amdgcn_s_setprio(3);
    }
    if constexpr (t >= T_GB && t < T_GB + 4) {
      constexpr int e = t - T_GB;
      const int q = 2 * al + (e >> 1);
      PIN2(la, lb);
      cf u = cpow(la, lb, (e & 1) ? 512.f * q : q ? 16.f * q : 1.f);
      if (e == 0) lam = u;
      if (e == 1 && al == 0) {
        const float lr = bf2f(q_lr), li = bf2f(q_li), nr = lam[0] - 1.0f, id = __builtin_amdgcn_rcpf(lr * lr + li * li);
        u = cf{(nr * lr + lam[1] * li) * id, (lam[1] * lr - nr * li) * id};
      }
      lds_put64(SC_LG + lane * 64 + q * 16 + (e & 1) * 8, __builtin_bit_cast(u32x2, u));
      if (e == 3) asm volatile("s_waitcnt lgkmcnt(0)" ::: "memory");
    }
    if constexpr (t == T_P1) { if (pub) st_u64(fme + FL_H1 * 32768, tag); }
    if constexpr (t == T_E0 - 3) pk = peek(f1 + FL_H1 * 32768);
    if constexpr (t == T_E0 - 1) {
      settle<0>(pk);
      early = wall(!use1 || pk == tag);
      if (early) { ldh1(0); ldh1(1); } else pk2 = peek(f1 + FL_H1 * 32768);
    }
    if constexpr (t == T_E0) {
      if (!early) { settle<0>(pk2); spin(f1 + FL_H1 * 32768, use1, pk2, tag); ldh1(0); ldh1(1); }
      ldh1(2); ldh1(3);
    }
    if constexpr (t == T_X1) {
      if (pub) st_u64(fme + FL_X1 * 32768, tag);
#pragma unroll 1
      for (u32 sp = 0; sp < SPIN_CAP; ++sp) {
        u64 v = peek(fx); settle<0>(v);
        miss = __builtin_amdgcn_ballot_w64(v != tag);
        if (!(miss & 0x0eull)) break;
        __builtin_amdgcn_s_sleep(2);
      }
      ldx1(1); ldx1(2); ldx1(3);
    }
    if constexpr (t == T_A) {
      if (miss & 0xf0ull) spin(fx, lane >= 4 && lane < 8, ~tag, tag);
      ldx1(4); ldx1(5);
    }
    if constexpr (t == T_A + 1) { ldx1(6); ldx1(7); }
    if constexpr (t == T_PH) { if (pub) st_u64(fme + FL_H * 32768, tag); }
    if constexpr (t == T_H - 2) pk = peek(f0 + FL_H * 32768);
    if constexpr (t == T_H) { settle<0>(pk); spin(f0 + FL_H * 32768, use0, pk, tag);
      if (c == 31 && j == 0 && pub) st_u64(ws + WS_EPOCH, tag);
    }
    if constexpr (t == T_P3) { if (pub) st_u64(fme + FL_H3 * 32768, tag); }
    if constexpr (t == T_P3Q) { if (pub) st_u64(fme + FL_H3Q * 32768, tag); }
    if constexpr (t == T_H3 - 1) pk = peek(f0 + FL_H3 * 32768);
    if constexpr (t == T_H3) { settle<8>(pk); spin(f0 + FL_H3 * 32768, use0, pk, tag); }
    if constexpr (t == T_H3Q - 2) pk = peek(f0 + FL_H3Q * 32768);
    if constexpr (t == T_H3Q) { settle<16>(pk); spin(f0 + FL_H3Q * 32768, use0, pk, tag); }
    sfor<a_issued(t - 1), a_issued(t)>([&](auto Q) {
      constexpr int q = Q.v;
      const int so = q < 8 ? WS_H + c * 131072 + q * GRP : WS_H3 + c * 229376 + (al ? g4_unit(q - 8, 1) - 1 : g4_unit(q - 8, 0)) * 4096;
      const int s1 = q < 8 ? so + SLOT : WS_H3 + c * 229376 + (al ? g4_unit(q - 8, 3) - 1 : g4_unit(q - 8, 2)) * 4096;
      dma4<SC1>(rw, lo + (q & 3) * GRP, vl, vl, vl, vl, so);
      dma4<SC1>(rw, lo + (q & 3) * GRP + SLOT, vl, vl, vl, vl, s1);
    });
  });
}

#define WADR(lo_, s) ((s) % NWG == 4 ? lo_##h : lo_ + wgr(s))
#define LANEVARS(ln)                                                                                                 \
  int ln = lane; asm volatile("" : "+v"(ln)::"memory");                                                              \
  const int i16 = ln & 15, g = ln >> 4, l31 = ln & 31, hh = ln >> 5, qq = (ln & 15) >> 2, pp = ln & 3, blk = g & 1;  \
  (void)i16; (void)l31; (void)hh; (void)qq; (void)pp; (void)blk

#define MM8(k) do { PIN1(a); a = mfma32(A[k], B[k], a); PIN1(a); } while (0)
template <bool FIRST, class RD> DI void step8f(f32x16& a, u32x4* A, u32x4* B, RD&& rd) {
  rd(0); rd(1); rd(2); rd(3);
  if (!FIRST) MM8(4);
  rd(4);
  if (!FIRST) MM8(5);
  rd(5);
  if (!FIRST) MM8(6);
  rd(6);
  if (!FIRST) MM8(7);
  rd(7);
  PIN1(a);
  a = mfma32(A[0], B[0], a); a = mfma32(A[1], B[1], a); a = mfma32(A[2], B[2], a); a = mfma32(A[3], B[3], a);
  PIN1(a);
}
template <bool FIRST, class RD> DI void step8(f32x16& a, u32x4* A, u32x4* B, RD&& rd) {
  if (!FIRST) MM8(4);
  rd(0); rd(1);
  if (!FIRST) MM8(5);
  rd(2);
  if (!FIRST) MM8(6);
  rd(3); rd(4);
  if (!FIRST) MM8(7);
  rd(5);
  MM8(0);
  rd(6);
  MM8(1);
  rd(7);
  PIN1(a);
  a = mfma32(A[2], B[2], a); a = mfma32(A[3], B[3], a);
  PIN1(a);
}
DI void tail8(f32x16& a, u32x4* A, u32x4* B) {
  a = mfma32(A[4], B[4], a); a = mfma32(A[5], B[5], a); a = mfma32(A[6], B[6], a); a = mfma32(A[7], B[7], a);
  PIN1(a);
}
template <bool FIRST, bool PRE, bool NEXT, class RW>
DI void step3(f32x16& g0, f32x16& g1, u32x4* X0, u32x4* X1, u32x4* W, u32 ab, u32 nx, RW&& rw) {
#define M30(k) do { PIN1(g0); g0 = mfma32(W[k], X0[k], g0); PIN1(g0); } while (0)
#define M31(k) do { PIN1(g1); g1 = mfma32(W[k], X1[k], g1); PIN1(g1); } while (0)
  if (!FIRST) M30(2);
  W[0] = rw(0);
  if (!PRE) { X0[0] = lds128(ab); X1[0] = lds128(ab + 512); X0[1] = lds128(ab + 2048); X1[1] = lds128(ab + 2048 + 512); }
  X0[2] = lds128(ab + 4096);
  if (!FIRST) M31(2);
  W[1] = rw(1); X1[2] = lds128(ab + 4096 + 512);
  if (!FIRST) M30(3);
  W[2] = rw(2); X0[3] = lds128(ab + 6144);
  if (!FIRST) M31(3);
  W[3] = rw(3); X1[3] = lds128(ab + 6144 + 512);
  if (FIRST) { asm volatile("" ::: "memory"); g0 = mfma32(W[0], X0[0], g0); PIN1(g0); } else M30(0);
  if (NEXT) X0[0] = lds128(nx);
  if (FIRST) { asm volatile("" ::: "memory"); g1 = mfma32(W[0], X1[0], g1); PIN1(g1); } else M31(0);
  if (NEXT) X1[0] = lds128(nx + 512);
  M30(1);
  if (NEXT) X0[1] = lds128(nx + 2048);
  M31(1);
  if (NEXT) X1[1] = lds128(nx + 2048 + 512);
}
DI void tail3(f32x16& g0, f32x16& g1, u32x4* X0, u32x4* X1, u32x4* W) {
  g0 = mfma32(W[2], X0[2], g0); g1 = mfma32(W[2], X1[2], g1); g0 = mfma32(W[3], X0[3], g0); g1 = mfma32(W[3], X1[3], g1);
  PIN2(g0, g1);
}
#define PIN8(x) asm volatile("" : "+v"(x[0]), "+v"(x[1]), "+v"(x[2]), "+v"(x[3]), "+v"(x[4]), "+v"(x[5]), "+v"(x[6]), "+v"(x[7]))
DI void gelu8a(float* u, float* e, f32x4 a0, f32x4 a1, f32x4 c0, f32x4 c1, f32x4 st) {
#pragma unroll
  for (int r = 0; r < 8; ++r) u[r] = __builtin_fmaf(st[0], r < 4 ? c0[r & 3] : c1[r & 3], r < 4 ? a0[r & 3] : a1[r & 3]);
  PIN8(u);
#pragma unroll
  for (int r = 0; r < 8; ++r) e[r] = u[r] * u[r];
  PIN8(e);
#pragma unroll
  for (int r = 0; r < 8; ++r) e[r] = __builtin_fmaf(e[r], st[2], st[1]);
  PIN8(e);
#pragma unroll
  for (int r = 0; r < 8; ++r) e[r] *= u[r];
  PIN8(e);
}
DI u32x4 gelu8b(float* u, float* e, f32x4 st) {
#pragma unroll
  for (int r = 0; r < 8; ++r) e[r] = __builtin_amdgcn_exp2f(e[r]);
  PIN8(e);
#pragma unroll
  for (int r = 0; r < 8; ++r) e[r] = __builtin_fmaf(e[r], st[3], st[3]);
  PIN8(e);
#pragma unroll
  for (int r = 0; r < 8; ++r) e[r] = __builtin_amdgcn_rcpf(e[r]);
  PIN8(e);
#pragma unroll
  for (int r = 0; r < 8; ++r) u[r] *= e[r];
  PIN8(u);
  return u32x4{pack2(u[0], u[1]), pack2(u[2], u[3]), pack2(u[4], u[5]), pack2(u[6], u[7])};
}
DI u32x4 gelu8(f32x4 a0, f32x4 a1, f32x4 c0, f32x4 c1, f32x4 st) {
  float u[8], e[8];
  gelu8a(u, e, a0, a1, c0, c1, st);
  return gelu8b(u, e, st);
}
DI f32x4 quad(const f32x16& a, int q) { return f32x4{a[4 * q], a[4 * q + 1], a[4 * q + 2], a[4 * q + 3]}; }
DI void smad(cf l, float& hr, float& hi, float br, float bi) {
  const float nr = __builtin_fmaf(l[0], hr, __builtin_fmaf(-l[1], hi, br));
  hi = __builtin_fmaf(l[0], hi, __builtin_fmaf(l[1], hr, bi));
  hr = nr;
}
DI f32x4 ldf4(u32 a) { return *(const LAS f32x4*)(a); }
DI f32x16 zero16() { f32x16 z; for (int r = 0; r < 16; ++r) z[r] = 0.f; return z; }

DI void group_p(const Params& p, int pw, int lane, int b, int c, int j) {
  const int nh = pw >> 1, th = pw & 1;
  const rsrc_t rw = mkrsrc(p.ws, WS_END);
  u32 wt, wth, xa[4], a2o, gea, csa, bbo, bbx, bboh, bbxh, xb;
  int so12;
  {
    LANEVARS(ln);
    const int tok = 32 * th + l31;
    wt = LDS_W + (8 * hh + qq) * 128 + 64 * (nh ^ (qq >> 1)) + 32 * blk + 16 * (pp & 1) + 8 * (pp >> 1); wth = wt + 4 * GRP;
    xa[0] = tok * 128 + ((hh ^ swx(tok)) << 4); xa[1] = xa[0] ^ 32; xa[2] = xa[0] ^ 64; xa[3] = xa[0] ^ 96;
    a2o = hh * 1024 + tok * 16;
    so12 = c * 65536 + j * 8192 + 4096 * nh + a2o;
    gea = SC_GEL + tok * 16; csa = SC_CS1 + (32 * nh + 8 * hh) * 4;
    bbo = LDS_W + (16 * pw + i16) * 128 + ((g ^ ((i16 >> 1) & 7)) << 4); bbx = bbo ^ 64; bboh = bbo + 4 * GRP; bbxh = bbx + 4 * GRP;
    PIN6(wt, wth, xa[0], xa[1], xa[2], xa[3]); PIN4(a2o, so12, gea, csa); PIN4(bbo, bbx, bboh, bbxh);
    const int t0 = 32 * th + 4 * hh;
    xb = LDS_A + j * SLOT + t0 * 128 + (((4 * nh + (l31 >> 3)) ^ swx(t0)) << 4) + (l31 & 7) * 2;
    PIN1(xb);
  }
  u32x4 A[8], B[8];
  f32x16 a = zero16();
  sfor<0, 4>([&](auto SS) {
    constexpr int s = SS.v;
    bar();
    step8f<s == 0>(a, A, B, [&](int k) {
      B[k] = lds128(LDS_A + s * GRP + (k >> 2) * SLOT + xa[k & 3]);
      A[k] = ldstr2(wt + wgr(s) + k * 2048, wt + wgr(s) + k * 2048 + 512);
    });
  });
  tail8(a, A, B);
  *(LAS f32x4*)(SC_X1 + pw * 2048 + lane * 16) = f32x4{a[8], a[9], a[10], a[11]};
  *(LAS f32x4*)(SC_X1 + pw * 2048 + lane * 16 + 1024) = f32x4{a[12], a[13], a[14], a[15]};
  u32 xp[8];
  u32 w3o, w3oh, q_dk, xu[16];
  u32x4 xr[2];
  {
    const u32x4 ones = {ONES, ONES, ONES, ONES};
    f32x4 sr = {0, 0, 0, 0}, si = sr, s2 = sr, s3 = sr;
    u32x4 r0, r1, i0, i1;
    auto sums = [&]() {
      PIN4(sr, si, s2, s3);
      sr = mfma16(ones, r0, sr); si = mfma16(ones, i0, si); s2 = mfma16(ones, r1, s2); s3 = mfma16(ones, i1, s3);
      PIN4(sr, si, s2, s3);
    };
    __builtin_amdgcn_s_setprio(2);
    sfor<0, 8>([&](auto SS) {
      constexpr int s = SS.v;
      if (T_GB + s == T_P1) bard(); else bar();
      if (s) sums();
      r0 = lds128(WADR(bbo, P_GB + s)); r1 = lds128(WADR(bbx, P_GB + s));
      i0 = lds128(WADR(bbo, P_GB + s) + SLOT); i1 = lds128(WADR(bbx, P_GB + s) + SLOT);
      if (s == 0) {
        const f32x4 st = ldf4(gea);
        st128(gelu8(quad(a, 0), quad(a, 1), ldf4(csa) + ldf4(csa + 256), ldf4(csa + 16) + ldf4(csa + 272), st), rw, WS_H1 + so12);
      }
      if (s == 1) {
        xr[0] = lds128(LDS_A + j * SLOT + (xa[0] ^ (64 * nh))); xr[1] = lds128(LDS_A + j * SLOT + (xa[1] ^ (64 * nh)));
      }
      if (s == 2) {
        q_dk = ((const GAS u16*)p.dsk)[64 * j + 32 * nh + (lane & 31)];
        if (pw == 0) slow_pull(p.w2, D * D * 2, b >> 3, 16, lane);
      }
      if (s >= 2 && s < 6) {
        constexpr int q = s >= 2 && s < 6 ? s - 2 : 0;
        const u32 lo_ = (xb ^ ((q >> 1) * 32)) + q * 1024, hi_ = (xb ^ ((q >> 1) * 32) ^ 16) + q * 1024 + 256;
        xu[4 * q] = lds16(lo_); xu[4 * q + 1] = lds16(lo_ + 128); xu[4 * q + 2] = lds16(hi_); xu[4 * q + 3] = lds16(hi_ + 128);
      }
    });
    sums();
    *(LAS f32x2*)(SC_CSB + (16 * pw + (lane & 15)) * 8) = f32x2{sr[0] + s2[0], si[0] + s3[0]};
    __builtin_amdgcn_s_setprio(0);
  }
  bar();
  {
    LANEVARS(ln);
    const LAS float* sp_ = (const LAS float*)(SC_STAT + ((gea - SC_GEL) >> 2));
    const float rs = sp_[64], mr = -sp_[0] * rs;
    PIN2(xr[0], xr[1]);
#pragma unroll
    for (int r = 0; r < 8; ++r) {
      a[2 * r] = __builtin_fmaf(bflo(xr[r >> 2][r & 3]), rs, mr); a[2 * r + 1] = __builtin_fmaf(bfhi(xr[r >> 2][r & 3]), rs, mr);
      PIN2(xu[2 * r], xu[2 * r + 1]);
      xp[r] = xu[2 * r] | (xu[2 * r + 1] << 16);
      PIN1(xp[r]);
    }
    w3o = LDS_W + (8 * hh + qq) * 256 + (pw ^ qq) * 64 + 32 * blk + 16 * (pp & 1) + 8 * (pp >> 1); w3oh = w3o + 4 * GRP;
    so12 += WS_X1;
    PIN4(w3o, w3oh, so12, a);
  }
  sfor<0, 4>([&](auto SS) {
    constexpr int s = SS.v;
    bar();
    step8<s == 0>(a, A, B, [&](int k) {
      B[k] = lds128(LDS_A + s * GRP + k * 2048 + a2o);
      A[k] = ldstr2(WADR(wt, P_G2 + s) + k * 2048, WADR(wt, P_G2 + s) + k * 2048 + 512);
    });
  });
  tail8(a, A, B);
  {
    const u32x4 v0 = {pack2(a[0], a[1]), pack2(a[2], a[3]), pack2(a[4], a[5]), pack2(a[6], a[7])};
    st128(v0, rw, so12);
    const u32x4 v1 = {pack2(a[8], a[9]), pack2(a[10], a[11]), pack2(a[12], a[13]), pack2(a[14], a[15])};
    st128(v1, rw, so12 + 2048);
    *(LAS u32x4*)(SC_X1 + 4096 * nh + a2o) = v0; *(LAS u32x4*)(SC_X1 + 4096 * nh + a2o + 2048) = v1;
  }
  bar();
  const int tile = pw + 3 * (j & 1);
  u32 a3o = a2o - 512 * th, aco, bc0;
  PIN1(a3o);
  f32x16 g0 = zero16(), g1 = zero16();
  {
    u32 a3s = a3o + SC_X1;
    PIN1(a3s);
    step3<true, false, false>(g0, g1, A, B, A + 4, a3s, 0, [&](int k) { return ldstr2(WADR(w3o, P_PR) + k * 4096, WADR(w3o, P_PR) + k * 4096 + 1024); });
    bard();
    {
      LANEVARS(ln);
      const int q = 2 * blk + (pp >> 1), n = 32 * nh + l31;
      aco = th * 1024 + q * 256 + (((8 * hh + qq) ^ (q << 2)) << 4) + (pp & 1) * 8;
      bc0 = LDS_W + n * 128 + ((hh ^ ((n >> 1) & 7)) << 4);
      PIN2(aco, bc0);
    }
    sfor<1, 8>([&](auto SS) {
      constexpr int s = SS.v;
      if (s > 1) barn(4); else bar();
      step3<false, (s > 1), (s < 7)>(g0, g1, A, B, A + 4, LDS_A + s * SLOT + a3o, LDS_A + (s + 1) * SLOT + a3o, [&](int k) {
        return ldstr2(WADR(w3o, pw_grp(s)) + k * 4096, WADR(w3o, pw_grp(s)) + k * 4096 + 1024);
      });
    });
    tail3(g0, g1, A, B, A + 4);
  }
  u32 acx = aco ^ 64, bc1 = bc0 ^ 32, bc2 = bc0 ^ 64, bc3 = bc0 ^ 96;
  u32 bc0h = bc0 + 4 * GRP, bc1h = bc1 + 4 * GRP, bc2h = bc2 + 4 * GRP, bc3h = bc3 + 4 * GRP;
  {
    int h3o = WS_H3 + c * 229376 + j * 28672 + tile * 4096 + a3o;
    const u32 cva = SC_CS3 + tile * 128 + ((csa - SC_CS1) & 63), ge0 = gea - 512 * th;
    f32x4 cv[4], st[2];
    PIN4(acx, bc1, bc2, bc3); PIN4(bc0h, bc1h, bc2h, bc3h); PIN1(h3o);
    float u[8], e[8];
    sfor<0, 8>([&](auto Q) {
      constexpr int q = Q.v, sx = (q >> 1) & 1;
      bar();
      if (q == 0) { st[0] = ldf4(ge0); cv[0] = ldf4(cva); cv[1] = ldf4(cva + 16); cv[2] = ldf4(cva + 64); cv[3] = ldf4(cva + 80); st[1] = ldf4(ge0 + 512); }
      if (!(q & 1)) {
        if (q < 4) PIN1(g0); else PIN1(g1);
        gelu8a(u, e, quad(q < 4 ? g0 : g1, 2 * sx), quad(q < 4 ? g0 : g1, 2 * sx + 1), cv[2 * sx], cv[2 * sx + 1], st[q >> 2]);
      } else {
        PIN8(e);
        st128(gelu8b(u, e, st[q >> 2]), rw, h3o + sx * 2048 + (q >> 2) * 512);
      }
    });
  }
  f32x16 y = zero16(), z;
  u32 b4o, b4oh, x1a;
  int oo;
  sfor<0, 8>([&](auto SS) {
    constexpr int s = SS.v;
    if (T_GC + s == T_P3) bard(); else bar();
    step8<s == 0>(y, A, B, [&](int k) {
      A[k] = ldstr2(LDS_A + (s & 3) * GRP + k * 2048 + aco, LDS_A + (s & 3) * GRP + k * 2048 + acx);
      B[k] = lds128(((k & 3) == 0 ? WADR(bc0, P_GC + s) : (k & 3) == 1 ? WADR(bc1, P_GC + s) : (k & 3) == 2 ? WADR(bc2, P_GC + s) : WADR(bc3, P_GC + s)) + (k >> 2) * SLOT);
    });
    if (s >= 1 && s < 5) {
#pragma unroll
      for (int r = 4 * (s - 1); r < 4 * s; ++r) z[r] = bf2f(lds16(x1a + (8 * (r >> 2) + (r & 3)) * 16));
      PIN1(z);
    }
    if (s == 0) {
      LANEVARS(ln);
      x1a = SC_X1 + (4 * nh + (l31 >> 3)) * 1024 + (32 * th + 4 * hh) * 16 + (l31 & 7) * 2;
      PIN1(x1a);
      b4o = LDS_W + (8 * hh + qq) * 128 + 64 * (nh ^ (qq >> 1)) + 32 * blk + 8 * pp; b4oh = b4o + 4 * GRP;
      oo = ((c * 64 + 32 * th + 4 * hh) * D + 64 * j + 32 * nh + l31) * 4;
      PIN3(b4o, b4oh, oo);
    }
  });
  tail8(y, A, B);
  sfor<0, 14>([&](auto SS) {
    constexpr int s = SS.v;
    bar();
    step8<s == 0>(z, A, B, [&](int k) {
      A[k] = lds128(LDS_A + (s & 3) * GRP + k * 2048 + a2o);
      B[k] = ldstr2(WADR(b4o, P_G4 + s) + k * 2048, WADR(b4o, P_G4 + s) + k * 2048 + 512);
    });
    if (s >= 3 && s < 7) {
      constexpr int q = s >= 3 && s < 7 ? s - 3 : 0;
      const float hd = 0.5f * bf2f(q_dk);
      const u32 sa = SC_STAT + (32 * th + 4 * (lane >> 5) + 8 * q) * 4;
      const f32x4 s0 = ldf4(sa), s1 = ldf4(sa + 256);
      y[4 * q] = __builtin_fmaf((bflo(xp[2 * q]) - s0[0]) * s1[0], hd, y[4 * q]);
      y[4 * q + 1] = __builtin_fmaf((bfhi(xp[2 * q]) - s0[1]) * s1[1], hd, y[4 * q + 1]);
      y[4 * q + 2] = __builtin_fmaf((bflo(xp[2 * q + 1]) - s0[2]) * s1[2], hd, y[4 * q + 2]);
      y[4 * q + 3] = __builtin_fmaf((bfhi(xp[2 * q + 1]) - s0[3]) * s1[3], hd, y[4 * q + 3]);
      PIN1(y);
    }
  });
  tail8(z, A, B);
  const rsrc_t ro = mkrsrc(p.out, S * D * 4);
#pragma unroll
  for (int h = 0; h < 2; ++h) {
    float e[8];
#pragma unroll
    for (int r = 0; r < 8; ++r) e[r] = -1.4426950408889634f * z[8 * h + r];
    PIN8(e);
#pragma unroll
    for (int r = 0; r < 8; ++r) e[r] = __builtin_amdgcn_exp2f(e[r]);
    PIN8(e);
#pragma unroll
    for (int r = 0; r < 8; ++r) e[r] = __builtin_fmaf(e[r], 0.5f, 0.5f);
    PIN8(e);
#pragma unroll
    for (int r = 0; r < 8; ++r) e[r] = __builtin_amdgcn_rcpf(e[r]);
    PIN8(e);
#pragma unroll
    for (int q = 0; q < 8; ++q) {
      const int r = 8 * h + q;
      const float res = __builtin_fmaf(y[r] * z[r], e[q], (r & 1) ? bfhi(xp[r >> 1]) : bflo(xp[r >> 1]));
      __builtin_amdgcn_raw_buffer_store_b32(f2u(res), ro, oo + (r & 1) * 2048, (8 * (r >> 2) + (r & 2)) * D * 4, SC1);
    }
  }
}

DI void group_q(const Params& p, int qw, int lane, int b, int c, int j) {
  const u16* tsrc = ((kptr_t)__builtin_amdgcn_kernarg_segment_ptr())[(0xCCCCC65Au >> (4 * (b >> 5))) & 15];
  const int tbytes = qw >= 2 ? 0 : b < 96 ? D * D * 2 : D * F * 2, tblk = 2 * (b < 96 ? b & 31 : b - 96) + qw;
  const rsrc_t rw = mkrsrc(p.ws, WS_END);
  const u32x4 ones = {ONES, ONES, ONES, ONES};
  __builtin_amdgcn_s_setprio(1);
  u32 xq, xqx, wn, bbo, bbx, bboh, bbxh, n16, g1a, g1s, g1c;
  int g1o;
  bool dia, m1, m2;
  {
    LANEVARS(ln);
    const int t0 = 16 * qq + pp, n = 16 * qw + i16;
    xq = t0 * 128 + ((g ^ swx(t0)) << 4); xqx = xq ^ 64;
    wn = LDS_W + (qw >> 1) * SLOT + (8 * hh + qq) * 128 + 64 * ((qw & 1) ^ (qq >> 1)) + 32 * blk + 8 * pp;
    bbo = LDS_W + n * 128 + ((g ^ ((n >> 1) & 7)) << 4); bbx = bbo ^ 64; bboh = bbo + 4 * GRP; bbxh = bbx + 4 * GRP;
    n16 = n;
    dia = g == qq; m1 = pp & 1; m2 = pp & 2;
    const int tok = 32 * (qw & 1) + l31;
    g1a = SC_X1 + qw * 2048 + ln * 16; g1s = SC_GEL + tok * 16; g1c = SC_CS1 + (32 * (qw >> 1) + 16 + 8 * hh) * 4;
    g1o = WS_H1 + c * 65536 + j * 8192 + 4096 * (qw >> 1) + 2048 + hh * 1024 + tok * 16;
    PIN6(xq, xqx, wn, bbo, bbx, bboh); PIN6(bbxh, n16, g1a, g1s, g1c, g1o);
  }
  {
    f32x4 sm = {0, 0, 0, 0}, gr = sm;
    f32x16 cs = zero16();
    const u32 xs = ((qw & 1) ? xqx : xq) + 512 * qw, xsx = xs ^ 64;
    u32x4 X[2][4], W[2][4];
    auto mq = [&](int h) {
      PIN2(sm, gr);
#pragma unroll
      for (int k = 0; k < 4; ++k) { sm = mfma16(ones, X[h][k], sm); gr = mfma16(X[h][k], X[h][k], gr); }
      PIN3(sm, gr, cs);
#pragma unroll
      for (int k = 0; k < 4; ++k) cs = mfma32(ones, W[h][k], cs);
    };
    u32 fa, fc;
    {
      LANEVARS(ln);
      fa = (16 * qq + 4 * qw + pp) * 4;
      fc = SC_CS1 + (qw >> 1) * 256 + (32 * (qw & 1) + l31) * 4;
      PIN2(fa, fc);
    }
    sfor<0, 4>([&](auto SS) {
      constexpr int s = SS.v;
      bar();
#pragma unroll
      for (int k = 0; k < 4; ++k) X[s & 1][k] = lds128(LDS_A + s * GRP + (k >> 1) * SLOT + ((k & 1) ? xsx : xs));
#pragma unroll
      for (int k = 0; k < 4; ++k) W[s & 1][k] = ldstr2(wn + wgr(s) + k * 2048, wn + wgr(s) + k * 2048 + 512);
      mq(s & 1);
      if (s < 3) PIN1(cs);
      if (s == 2) l2_pull<1>(tsrc, tbytes, tblk, lane);
    });
    const float dlo = m1 ? gr[1] : gr[0], dhi = m1 ? gr[3] : gr[2], dg = m2 ? dhi : dlo;
    const float mean = sm[0] * (1.0f / D);
    const float var = __builtin_fmaxf(__builtin_fmaf(dg, 1.0f / D, -mean * mean), 0.f);
    const float rstd = __builtin_amdgcn_rsqf(var + EPS);
    const float r12 = rstd * __builtin_amdgcn_rsqf(__builtin_fmaf(var * rstd, rstd, EPS));
    if (dia) {
      *(LAS f32x4*)(SC_GEL + fa * 4) = f32x4{-mean, GC0 * r12, GC1 * r12 * r12 * r12, __builtin_amdgcn_rcpf(r12)};
      *(LAS float*)(SC_STAT + fa) = mean; *(LAS float*)(SC_STAT + 256 + fa) = rstd;
    }
    PIN1(cs);
    *(LAS float*)(fc) = cs[0];
  }
  f32x4 ar[4], ai[4], mn[4], rs[4];
#pragma unroll
  for (int q = 0; q < 4; ++q) ar[q] = ai[q] = f32x4{0, 0, 0, 0};
  {
    u32x4 X[4][4], FR[4], FI[4];
    auto m1_ = [&](int h, int i, bool pin) {
      const int hb = h + (i >> 3), q = (i >> 1) & 3;
      if (i & 1) { if (pin) PIN1(ai[q]); ai[q] = mfma16(X[hb][q], FI[hb], ai[q]); PIN1(ai[q]); }
      else { if (pin) PIN1(ar[q]); ar[q] = mfma16(X[hb][q], FR[hb], ar[q]); PIN1(ar[q]); }
    };
    sfor<0, 9>([&](auto SS) {
      constexpr int s = SS.v, h = 2 * (s & 1), o = h ^ 2;
      constexpr u32 ab = LDS_A + s * SLOT;
      if (T_GB + s == T_P1) bard(); else bar();
      f32x4 a0, a1, st, c0, c1, c2, c3;
      if (s == 0) {
        a0 = ldf4(g1a); a1 = ldf4(g1a + 1024); st = ldf4(g1s);
        c0 = ldf4(g1c); c2 = ldf4(g1c + 256); c1 = ldf4(g1c + 16); c3 = ldf4(g1c + 272); asm volatile("" ::: "memory");
      }
      if (s == 8) {
#pragma unroll
        for (int r = 0; r < 4; ++r) { mn[r] = ldf4(SC_STAT + (16 * (lane >> 4) + 4 * r) * 4); rs[r] = ldf4(SC_STAT + 256 + (16 * (lane >> 4) + 4 * r) * 4); }
      }
#pragma unroll
      for (int i = 0; i < 16; ++i) {
        if (s) m1_(o, i, s > 1 || i >= 8);
        if (s < 8 && i < 12) {
          const int k = i / 6, r = i % 6;
          if (r == 1) FR[h + k] = lds128(k ? WADR(bbx, P_GB + s) : WADR(bbo, P_GB + s));
          else if (r == 2) FI[h + k] = lds128((k ? WADR(bbx, P_GB + s) : WADR(bbo, P_GB + s)) + SLOT);
          else { const int q = r ? r - 2 : 0; X[h + k][q] = lds128(ab + 512 * q + (((q ^ k) & 1) ? xqx : xq)); }
        }
      }
      if (s == 0) {
        asm volatile("" : "+v"(c0), "+v"(c1)::"memory");
        st128(gelu8(a0, a1, c0 + c2, c1 + c3, st), rw, g1o);
      }
    });
  }
  float br[16], bi[16];
  cf lam, coef;
  u32x2 tg;
  float prr, pri;
  u32x4 WR[16];
  u32 w3o, w3oh;
  int hso;
  auto rdq = [&](u32 base, int k) { return ldstr2(base + (k >> 1) * SLOT + (k & 1) * 3072, base + (k >> 1) * SLOT + (k & 1) * 3072 + 768); };
  auto ldq = [&](auto Q) {
    constexpr int q = Q.v;
#pragma unroll
    for (int k = 0; k < 4; ++k) WR[4 * q + k] = rdq(WADR(w3o, P_QR + q), k);
  };
  {
    LANEVARS(ln);
    const f32x4 t0 = ldf4(SC_LG + n16 * 64);
    const cf csb = *(const LAS f32x2*)(SC_CSB + n16 * 8), l16 = *(const LAS f32x2*)(SC_LG + n16 * 64 + 16);
    tg = lds64(SC_TAG);
    lam = cf{t0[0], t0[1]};
    coef = cf{t0[2], t0[3]};
    w3o = LDS_W + (8 * hh + qq) * 192 + 64 * (qw < 3 ? qw : 0) + 32 * blk + 16 * (pp & 1) + 8 * (pp >> 1); w3oh = w3o + 4 * GRP;
    float er = 0.f, ei = 0.f;
#pragma unroll
    for (int r = 0; r < 16; ++r) {
      br[r] = __builtin_fmaf(-mn[r >> 2][r & 3], csb[0], ar[r >> 2][r & 3]) * rs[r >> 2][r & 3];
      bi[r] = __builtin_fmaf(-mn[r >> 2][r & 3], csb[1], ai[r >> 2][r & 3]) * rs[r >> 2][r & 3];
      if (r) smad(lam, er, ei, br[r], bi[r]); else { er = br[0]; ei = bi[0]; }
      if (r == 7) { PIN4(er, ei, w3o, w3oh); bar(); PIN4(ar[2], ai[2], er, ei); }
      if (r == 11) { PIN2(er, ei); bar(); sfor<0, 1>(ldq); PIN4(ar[3], ai[3], er, ei); }
    }
    PIN2(er, ei);
    bar();
    float v0r, v0i, v1r, v1i, v2r, v2i, v3r, v3i;
    {
      auto a = __builtin_amdgcn_permlane16_swap(f2u(er), f2u(er), false, false);
      auto a0 = __builtin_amdgcn_permlane32_swap(a[0], a[0], false, false), a1 = __builtin_amdgcn_permlane32_swap(a[1], a[1], false, false);
      auto d = __builtin_amdgcn_permlane16_swap(f2u(ei), f2u(ei), false, false);
      auto d0 = __builtin_amdgcn_permlane32_swap(d[0], d[0], false, false), d1 = __builtin_amdgcn_permlane32_swap(d[1], d[1], false, false);
      v0r = u2f(a0[0]); v0i = u2f(d0[0]); v1r = u2f(a1[0]); v1i = u2f(d1[0]);
      v2r = u2f(a0[1]); v2i = u2f(d0[1]); v3r = u2f(a1[1]); v3i = u2f(d1[1]);
    }
    float pr = v0r, pi = v0i;
    prr = g == 1 ? pr : 0.f; pri = g == 1 ? pi : 0.f;
    smad(l16, pr, pi, v1r, v1i);
    prr = g == 2 ? pr : prr; pri = g == 2 ? pi : pri;
    smad(l16, pr, pi, v2r, v2i);
    prr = g == 3 ? pr : prr; pri = g == 3 ? pi : pri;
    smad(l16, pr, pi, v3r, v3i);
    PIN2(pr, pi);
    st128(u32x4{f2u(pr), tg[0], f2u(pi), tg[1]}, rw, g == 0 ? WS_E + (c * 512 + 64 * j + n16) * 16 : OOB);
    PIN2(prr, pri);
    bar();
    hso = WS_H + c * 131072 + j * 16384 + (2 * qw + (g >> 1)) * 1024 + (g & 1) * 512 + ((i16 ^ ((g & 1) << 3)) << 4);
    PIN1(hso);
  }
  {
    LANEVARS(ln);
    const rsrc_t re_ = mkrsrc(p.ws + WS_E, 32 * 512 * 16);
    const int evo = (64 * j + n16) * 16 + (c - 1 - 8 * g) * 8192;
    const int c3o = WS_CS3 + (7 * j + 2 * qw) * 2048 + ln * 16, c3x = qw < 3 ? 2048 : 0;
    const int nv = c - 8 * g;
    u32x4 ge[8], cs[4];
    auto ld_e = [&]() {
      asm volatile("" ::: "memory");
#pragma unroll
      for (int d = 0; d < 8; ++d) ge[d] = __builtin_amdgcn_raw_buffer_load_b128(re_, evo - d * 8192, 0, SC1);
    };
    auto ld_c = [&]() {
      asm volatile("" ::: "memory");
#pragma unroll
      for (int i = 0; i < 4; ++i) cs[i] = __builtin_amdgcn_raw_buffer_load_b128(rw, c3o + (i & 1) * 1024, (i >> 1) * c3x, SC1);
    };
    ld_c();
    bar();
    sfor<1, 3>(ldq);
    ld_e();
    bar();
#pragma unroll 1
    for (u32 sp = 0; sp < SPIN_CAP; ++sp) {
      u32 bad = 0, bad2 = 0;
#pragma unroll
      for (int i = 0; i < 4; ++i) { bad = xor_or(cs[i][1], tg[0], bad); bad2 = xor_or(cs[i][3], tg[1], bad2); }
      if (wall((bad | bad2) == 0)) break;
      ld_c();
    }
#pragma unroll
    for (int q = 0; q < 2; ++q) {
      LAS float* cp = (LAS float*)(SC_CS3 + (2 * qw + q) * 128 + (16 * (i16 >> 3) + (i16 & 7)) * 4);
      cp[0] = xsum32(xsum16(u2f(cs[2 * q][0]) + u2f(cs[2 * q + 1][0])));
      cp[8] = xsum32(xsum16(u2f(cs[2 * q][2]) + u2f(cs[2 * q + 1][2])));
    }
#pragma unroll 1
    for (u32 sp = 0; sp < SPIN_CAP; ++sp) {
      u32 bad = 0, bad2 = 0;
#pragma unroll
      for (int d = 0; d < 8; ++d) { bad = xor_or(ge[d][1], d < nv ? tg[0] : 0u, bad); bad2 = xor_or(ge[d][3], d < nv ? tg[1] : 0u, bad2); }
      if (wall((bad | bad2) == 0)) break;
      ld_e();
    }
    f32x4 lg = ldf4(SC_LG + n16 * 64 + g * 16);
    if (g == 0) lg = f32x4{1.f, 0.f, 1.f, 0.f};
    const cf l32 = *(const LAS f32x2*)(SC_LG + n16 * 64 + 32), l64 = cmul(l32, l32);
    cf part = {u2f(ge[7][0]), u2f(ge[7][2])};
#pragma unroll
    for (int d = 6; d >= 0; --d) part = cmad(l64, part, cf{u2f(ge[d][0]), u2f(ge[d][2])});
    part = cmul(cf{lg[2], lg[3]}, part);
    float hr_ = xsum32(xsum16(part[0])), hi_ = xsum32(xsum16(part[1]));
    smad(cf{lg[0], lg[1]}, hr_, hi_, prr, pri);
    PIN2(hr_, hi_);
    sfor<3, 4>(ldq);
#pragma unroll
    for (int u = 0; u < 2; ++u) {
      float hr[8], hi[8];
#pragma unroll
      for (int r = 0; r < 8; ++r) {
        smad(lam, hr_, hi_, br[8 * u + r], bi[8 * u + r]);
        hr[r] = __builtin_fmaf(coef[0], hr_, -coef[1] * hi_); hi[r] = __builtin_fmaf(coef[0], hi_, coef[1] * hr_);
      }
      st128(u32x4{pack2(hr[0], hr[1]), pack2(hr[2], hr[3]), pack2(hr[4], hr[5]), pack2(hr[6], hr[7])}, rw, (hso ^ (64 * u)) + 256 * u);
      st128(u32x4{pack2(-hi[0], -hi[1]), pack2(-hi[2], -hi[3]), pack2(-hi[4], -hi[5]), pack2(-hi[6], -hi[7])}, rw, (hso ^ (64 * u)) + 256 * u + 8192);
      PIN2(hr_, hi_);
    }
  }
  u32 a3o, cva, gea;
  int h3o;
  {
    LANEVARS(ln);
    const int tile = qw + 4 - 4 * (j & 1);
    a3o = hh * 1024 + l31 * 16;
    h3o = WS_H3 + c * 229376 + j * 28672 + tile * 4096 + a3o;
    cva = SC_CS3 + tile * 128 + 32 * hh; gea = SC_GEL + l31 * 16;
    u32 xs = g * 1024 + n16 * 16;
    PIN4(a3o, h3o, cva, gea); PIN1(xs);
    f32x4 sm = {0, 0, 0, 0}, gr = sm;
    auto slab = [&](int s) {
      const u32x4 x0 = lds128((s ? LDS_A + s * SLOT : SC_X1) + xs), x1 = lds128((s ? LDS_A + s * SLOT : SC_X1) + 4096 + xs);
      sm = mfma16(ones, x0, sm); gr = mfma16(x0, x0, gr); sm = mfma16(ones, x1, sm); gr = mfma16(x1, x1, gr);
      PIN2(sm, gr);
    };
    slab(0);
    sfor<1, 8>([&](auto SS) {
      constexpr int s = SS.v;
      if (T_A + s - 1 == T_PH) bard(); else bar();
      if (s == 1) slab(1);
      if (s < 7) slab(s + 1);
    });
    const float dlo = m1 ? gr[1] : gr[0], dhi = m1 ? gr[3] : gr[2], dg = m2 ? dhi : dlo;
    const float m = sm[0] * (1.0f / D), ve = __builtin_fmaxf(__builtin_fmaf(dg, 1.0f / D, -m * m), 0.f) + EPS, r = __builtin_amdgcn_rsqf(ve);
    if (dia) *(LAS f32x4*)(SC_GEL + n16 * 16) = f32x4{-m, GC0 * r, GC1 * r * r * r, ve * r};
  }
  if (qw == 3) {
    bar();
#pragma unroll
    for (int t = 0; t < 9; ++t) { if (t == 5) slow_pull(p.w4, D * F * 2, b >> 3, 56, lane); lbar(); }
    drain();
    return;
  }
  f32x16 g0 = zero16(), g1 = zero16();
  {
    u32x4 X0[4], X1[4], W[4];
    u32 a3s = a3o + SC_X1;
    PIN1(a3s);
    sfor<0, 8>([&](auto SS) {
      constexpr int s = SS.v;
      if (s) barn(4); else bar();
      step3<s == 0, (s > 0), (s < 7)>(g0, g1, X0, X1, W, s ? LDS_A + s * SLOT + a3o : a3s, LDS_A + (s + 1) * SLOT + a3o, [&](int k) {
        if constexpr (s >= 4) return WR[4 * (s - 4) + k];
        else return rdq(WADR(w3o, P_QW + s), k);
      });
    });
    tail3(g0, g1, X0, X1, W);
  }
  {
    const f32x4 cv[4] = {ldf4(cva), ldf4(cva + 16), ldf4(cva + 64), ldf4(cva + 80)}, st[2] = {ldf4(gea), ldf4(gea + 512)};
    sfor<0, 4>([&](auto Q) {
      constexpr int q = Q.v, sx_ = q & 1;
      bar();
      if (q >> 1) PIN1(g1); else PIN1(g0);
      st128(gelu8(quad((q >> 1) ? g1 : g0, 2 * sx_), quad((q >> 1) ? g1 : g0, 2 * sx_ + 1), cv[2 * sx_], cv[2 * sx_ + 1], st[q >> 1]), rw, h3o + sx_ * 2048 + (q >> 1) * 512);
    });
  }
  bar(); bar();
  bard();
}

__global__ __launch_bounds__(NTHR, 3) void fused_kernel(Params) {
  extern __shared__ __attribute__((aligned(16))) char smem[];
  const int lane = __builtin_amdgcn_workitem_id_x() & 63;
  const int w = __builtin_amdgcn_readfirstlane(__builtin_amdgcn_workitem_id_x() >> 6);
  const int b = __builtin_amdgcn_workgroup_id_x();
  const int c = (b & 7) * 4 + (b >> 6), j = (b >> 3) & 7;
  int v0[4];
  if (w < 7) {
    if (w < 4) __builtin_amdgcn_s_setprio(3);
    const u16* src = ((kptr_t)__builtin_amdgcn_kernarg_segment_ptr())[(0x6c >> w) & 1 ? 9 : 0];
    if (w < 4) {
      const int ax = (w - 2) >> 31;
#pragma unroll
      for (int i = 0; i < 4; ++i) {
        const int t = 32 * (w & 1) + 8 * i + (lane >> 3);
        const int sa = swx(t), sb = ((lane >> 4) & 1) << 2;
        v0[i] = (t - i) * 1024 + (((lane & 7) ^ sb ^ ((sa ^ sb) & ax)) << 4);
      }
      const rsrc_t r0 = mkrsrc(src, w < 2 ? S * D * 2 : D * D * 2);
      const u32 l0 = (w < 2 ? LDS_A : LDS_W) + (w & 1) * 4096;
      const int s0 = w < 2 ? c * 65536 : 128 * j, ds = w < 2 ? 128 : 65536;
#pragma unroll
      for (int m = 0; m < 2 * NB0; ++m)
        asm volatile("s_mov_b32 m0, %0\n\ts_nop 0\n\tbuffer_load_dwordx4 %1, %5, %6 offen lds\n\tbuffer_load_dwordx4 %2, %5, %6 offen offset:1024 lds\n\t"
                     "buffer_load_dwordx4 %3, %5, %6 offen offset:2048 lds\n\tbuffer_load_dwordx4 %4, %5, %6 offen offset:3072 lds"
                     ::"s"(l0 + m * SLOT), "v"(v0[0]), "v"(v0[1]), "v"(v0[2]), "v"(v0[3]), "s"(r0), "s"(s0 + m * ds) : "memory", "m0");
    } else {
      const int o1 = ((w == 4 ? c * 8 + j : (b >> 3) * 2 + w - 5) * 64 + lane) * 128;
      asm volatile("s_mov_b32 m0, %0\n\ts_nop 0\n\tbuffer_load_dword %1, %2, 0 offen lds" ::"s"(SC_JUNK), "v"(o1), "s"(mkrsrc(src, S * D * 2)) : "memory", "m0");
    }
  }
  kptr_t ka = (kptr_t)__builtin_amdgcn_kernarg_segment_ptr();
  asm volatile("" : "+s"(ka)::"memory");
  Params p = {ka[0], ka[1], ka[2], ka[3], ka[4], ka[5], ka[6], ka[7], ka[8], ka[9], ka[10], ka[11], ka[12], (float*)ka[13], (char*)ka[14]};
  asm volatile("" : "+s"(p.x), "+s"(p.lre), "+s"(p.lim), "+s"(p.bre), "+s"(p.bim), "+s"(p.cre), "+s"(p.cim), "+s"(p.ldt), "+s"(p.dsk),
               "+s"(p.w1), "+s"(p.w2), "+s"(p.w3), "+s"(p.w4), "+s"(p.out), "+s"(p.ws));
  if (w < 2) act_loader(p, w, lane, b, c, j, v0);
  else if (w < 4) { __builtin_amdgcn_s_setprio(2); weight_loader(p, w - 2, lane, j, v0); }
  else if (w < 8) group_p(p, w - 4, lane, b, c, j);
  else group_q(p, w - 8, lane, b, c, j);
}
}

extern "C" void kernel_launch(void* const* d_in, const int* in_sizes, int n_in, void* d_out, int out_size, void* d_ws,
                              size_t ws_size, hipStream_t stream) {
  using namespace s5k;
  Params p;
  p.x = (const u16*)d_in[0]; p.lre = (const u16*)d_in[1]; p.lim = (const u16*)d_in[2];
  p.bre = (const u16*)d_in[3]; p.bim = (const u16*)d_in[4]; p.cre = (const u16*)d_in[5]; p.cim = (const u16*)d_in[6];
  p.ldt = (const u16*)d_in[7]; p.dsk = (const u16*)d_in[8];
  p.w1 = (const u16*)d_in[10]; p.w2 = (const u16*)d_in[11]; p.w3 = (const u16*)d_in[12]; p.w4 = (const u16*)d_in[13];
  p.out = (float*)d_out; p.ws = (char*)d_ws;
  (void)hipFuncSetAttribute((const void*)fused_kernel, hipFuncAttributeMaxDynamicSharedMemorySize, LDS_BYTES);
  fused_kernel<<<256, NTHR, LDS_BYTES, stream>>>(p);
}
#if defined(__HIP_DEVICE_COMPILE__)
#pragma clang attribute pop
#endif
```
